```python
import torch

def _softmax(x, axis=-1):
    return torch.softmax(x, dim=axis)
def _gelu(x):
    return torch.nn.functional.gelu(x, approximate="tanh")
def _silu(x):
    return torch.nn.functional.silu(x)
def _sigmoid(x):
    return torch.sigmoid(x)
def _layernorm(x, axis=-1, eps=1e-5):
    m = x.mean(dim=axis, keepdim=True)
    v = x.var(dim=axis, keepdim=True, unbiased=False)
    return (x - m) / torch.sqrt(v + eps)
def _scatter_add(tgt, idx, src, axis=0):
    return tgt.clone().index_add_(axis, idx, src)
def _segment_sum(src, seg, n):
    out = torch.zeros((n,) + src.shape[1:], dtype=src.dtype)
    return out.index_add_(0, seg, src)
def _pad(x, pw):
    flat = tuple(v for pair in reversed(pw) for v in pair)
    return torch.nn.functional.pad(x, flat)

kernel_name = 'parallel_sum__sliding_block_cached__mixer_channel_mlp__decode'

def setup_inputs():

    _w = (lambda a: (lambda t: t.to(torch.bfloat16) if t.is_floating_point() else t)(torch.as_tensor(a)))
    def _A_setup():
        B, Sq, D = 1, 1, 1024
        Hq, Hkv, Sk, Dh = 32, 4, 65536, 128
        assert Hq % Hkv == 0, (Hq, Hkv)
        return {
            "x": _w((lambda *s: torch.randn(*s, dtype=torch.bfloat16) * 0.05)(B, Sq, D)),
            "k_cache": _w((lambda *s: torch.randn(*s, dtype=torch.bfloat16) * 3.2)(B, Hkv, Sk, Dh)),
            "v_cache": _w((lambda *s: torch.randn(*s, dtype=torch.bfloat16) * 0.4)(B, Hkv, Sk, Dh)),
            "wq": _w((lambda *s: torch.randn(*s, dtype=torch.bfloat16) * 3.2)(D, Hq * Dh) * (D ** -0.5)),
            "wo": _w((lambda *s: torch.randn(*s, dtype=torch.bfloat16) * 0.4)(Hq * Dh, D) * ((Hq * Dh) ** -0.5)),
        }
    _A_in = _A_setup()

    def _B_setup():
        B, S, D, F = 1, 1, 1024, 3584
        return {
            "x": _w((lambda *s: torch.randn(*s, dtype=torch.bfloat16) * 0.05)(B, S, D)),
            "w1": _w((lambda *s: torch.randn(*s, dtype=torch.bfloat16) * 1.6)(D, F) * (D ** -0.5)),
            "b1": _w((lambda *s: torch.randn(*s, dtype=torch.bfloat16) * 1.6)(F)),
            "w2": _w((lambda *s: torch.randn(*s, dtype=torch.bfloat16) * 0.1)(F, D) * (F ** -0.5)),
            "b2": _w((lambda *s: torch.randn(*s, dtype=torch.bfloat16) * 0.1)(D)),
        }
    _B_in = _B_setup()

    return {
        "A_x": _A_in["x"],
        "A_k_cache": _A_in["k_cache"],
        "A_v_cache": _A_in["v_cache"],
        "A_wq": _A_in["wq"],
        "A_wo": _A_in["wo"],
        "B_x": _B_in["x"],
        "B_w1": _B_in["w1"],
        "B_b1": _B_in["b1"],
        "B_w2": _B_in["w2"],
        "B_b2": _B_in["b2"],
    }

def reference(A_x, A_k_cache, A_v_cache, A_wq, A_wo, B_x, B_w1, B_b1, B_w2, B_b2):
    def _A_ref(x, k_cache, v_cache, wq, wo):
        B, Sq, D = x.shape
        Hq, Dh = 32, 128
        Hkv, Sk = k_cache.shape[1], k_cache.shape[2]
        W = min(4096, max(256, Sk // 4))
        rep = Hq // Hkv
        q = (lambda x, *d: x.permute(*d))((x @ wq).reshape(B, Sq, Hq, Dh), 0, 2, 1, 3)
        k = (lambda x, n, axis: torch.repeat_interleave(x, n, dim=axis))(k_cache[:, :, Sk - W:, :], rep, axis=1)
        v = (lambda x, n, axis: torch.repeat_interleave(x, n, dim=axis))(v_cache[:, :, Sk - W:, :], rep, axis=1)
        scores = torch.einsum("bhqd,bhkd->bhqk", q, k) * (Dh ** -0.5)
        p = _softmax(scores, axis=-1)
        o = torch.einsum("bhqk,bhkd->bhqd", p, v)
        return (lambda x, *d: x.permute(*d))(o, 0, 2, 1, 3).reshape(B, Sq, Hq * Dh) @ wo

    def _B_ref(x, w1, b1, w2, b2):
        y = _layernorm(x, axis=-1)
        h = _gelu(torch.einsum("bsd,df->bsf", y, w1) + b1)
        return torch.einsum("bsf,fd->bsd", h, w2) + b2

    x = A_x
    xn = _layernorm(x, axis=-1)
    return x + _A_ref(xn, A_k_cache, A_v_cache, A_wq, A_wo) + _B_ref(xn, B_w1, B_b1, B_w2, B_b2)
```

```cpp
#include <hip/hip_runtime.h>
#include <hip/hip_bf16.h>
#include <stdint.h>

typedef unsigned v4u __attribute__((ext_vector_type(4)));
typedef unsigned short u16;

constexpr int D = 1024, HQ = 32, HKV = 4, SK = 65536, DH = 128, W = 4096, F = 3584;
constexpr int NSPLIT = 64, KPS = W / NSPLIT;

__device__ __forceinline__ float bf2f(u16 b) { return __builtin_bit_cast(float, (unsigned)b << 16); }
__device__ __forceinline__ float lo(unsigned w) { return __builtin_bit_cast(float, w << 16); }
__device__ __forceinline__ float hi(unsigned w) { return __builtin_bit_cast(float, w & 0xffff0000u); }
__device__ __forceinline__ float rbf(float f) { return __bfloat162float(__float2bfloat16(f)); }

__device__ float block_sum(float v, float* red) {
  for (int o = 32; o > 0; o >>= 1) v += __shfl_xor(v, o);
  __syncthreads();
  if ((threadIdx.x & 63) == 0) red[threadIdx.x >> 6] = v;
  __syncthreads();
  float s = 0.f;
  for (int i = 0; i < (int)(blockDim.x >> 6); ++i) s += red[i];
  return s;
}

__global__ void __launch_bounds__(256) k1(const u16* x, const u16* wq, const u16* w1, const u16* b1, float* q, float* h) {
  __shared__ float xs[D];
  __shared__ float red[8];
  __shared__ float acc_s[64][33];
  const int tid = threadIdx.x;
  const bool isq = blockIdx.x < 128;
  float v[4];
  float s = 0.f;
  for (int i = 0; i < 4; ++i) { v[i] = bf2f(x[tid * 4 + i]); s += v[i]; }
  float m = block_sum(s, red) / D;
  s = 0.f;
  for (int i = 0; i < 4; ++i) { float d = v[i] - m; s += d * d; }
  float var = block_sum(s, red) / D;
  float r = 1.f / sqrtf(var + 1e-5f);
  for (int i = 0; i < 4; ++i) v[i] = rbf((v[i] - m) * r);
  if (!isq) {
    s = 0.f;
    for (int i = 0; i < 4; ++i) s += v[i];
    m = block_sum(s, red) / D;
    s = 0.f;
    for (int i = 0; i < 4; ++i) { float d = v[i] - m; s += d * d; }
    var = block_sum(s, red) / D;
    r = 1.f / sqrtf(var + 1e-5f);
    for (int i = 0; i < 4; ++i) v[i] = rbf((v[i] - m) * r);
  }
  for (int i = 0; i < 4; ++i) xs[tid * 4 + i] = v[i];
  __syncthreads();
  const u16* wbase; int ld, c0;
  if (isq) { wbase = wq; ld = HQ * DH; c0 = blockIdx.x * 32; } else { wbase = w1; ld = F; c0 = (blockIdx.x - 128) * 32; }
  const int cc = (tid & 3) * 8, rr = tid >> 2;
  float a[8] = {0, 0, 0, 0, 0, 0, 0, 0};
#pragma unroll
  for (int p = 0; p < 16; ++p) {
    const int row = p * 64 + rr;
    v4u wv = __builtin_nontemporal_load((const v4u*)(wbase + (size_t)row * ld + c0 + cc));
    const float xv = xs[row];
    a[0] += xv * lo(wv.x); a[1] += xv * hi(wv.x); a[2] += xv * lo(wv.y); a[3] += xv * hi(wv.y);
    a[4] += xv * lo(wv.z); a[5] += xv * hi(wv.z); a[6] += xv * lo(wv.w); a[7] += xv * hi(wv.w);
  }
  for (int i = 0; i < 8; ++i) acc_s[rr][cc + i] = a[i];
  __syncthreads();
  if (tid < 32) {
    float t = 0.f;
    for (int i = 0; i < 64; ++i) t += acc_s[i][tid];
    if (isq) q[c0 + tid] = rbf(t);
    else {
      float u = t + bf2f(b1[c0 + tid]);
      float g = 0.5f * u * (1.f + tanhf(0.7978845608028654f * (u + 0.044715f * u * u * u)));
      h[c0 + tid] = g;
    }
  }
}

__global__ void __launch_bounds__(256) k2(const u16* kc, const u16* vc, const float* q, float* po, float* pml) {
  __shared__ float qs[8][DH];
  __shared__ float sc[8][KPS];
  __shared__ float ml[8][2];
  const int g = blockIdx.x / NSPLIT, sp = blockIdx.x % NSPLIT, tid = threadIdx.x;
  for (int i = tid; i < 8 * DH; i += 256) qs[i / DH][i % DH] = q[g * 8 * DH + i];
  __syncthreads();
  const u16* kb = kc + ((size_t)g * SK + (SK - W) + sp * KPS) * DH;
  const u16* vb = vc + ((size_t)g * SK + (SK - W) + sp * KPS) * DH;
  {
    const int key = tid >> 2, qt = tid & 3;
    float a[8] = {0, 0, 0, 0, 0, 0, 0, 0};
    for (int c = 0; c < 4; ++c) {
      v4u kv = *(const v4u*)(kb + key * DH + qt * 32 + c * 8);
      float kf[8] = {lo(kv.x), hi(kv.x), lo(kv.y), hi(kv.y), lo(kv.z), hi(kv.z), lo(kv.w), hi(kv.w)};
      for (int hh = 0; hh < 8; ++hh)
        for (int e = 0; e < 8; ++e) a[hh] += kf[e] * qs[hh][qt * 32 + c * 8 + e];
    }
    for (int hh = 0; hh < 8; ++hh) {
      a[hh] += __shfl_xor(a[hh], 1);
      a[hh] += __shfl_xor(a[hh], 2);
      if (qt == 0) sc[hh][key] = a[hh] * 0.08838834764831845f;
    }
  }
  __syncthreads();
  {
    const int wv = tid >> 6, lane = tid & 63;
    for (int j = 0; j < 2; ++j) {
      const int hh = wv * 2 + j;
      float s = sc[hh][lane];
      float m = s;
      for (int o = 32; o > 0; o >>= 1) m = fmaxf(m, __shfl_xor(m, o));
      float p = __expf(s - m);
      float l = p;
      for (int o = 32; o > 0; o >>= 1) l += __shfl_xor(l, o);
      sc[hh][lane] = p;
      if (lane == 0) { ml[hh][0] = m; ml[hh][1] = l; }
    }
  }
  __syncthreads();
  {
    const int dp = tid & 63, hp = tid >> 6;
    float a00 = 0, a01 = 0, a10 = 0, a11 = 0;
    for (int j = 0; j < KPS; ++j) {
      unsigned vv = *(const unsigned*)(vb + j * DH + dp * 2);
      float p0 = sc[hp * 2][j], p1 = sc[hp * 2 + 1][j];
      a00 += p0 * lo(vv); a01 += p0 * hi(vv); a10 += p1 * lo(vv); a11 += p1 * hi(vv);
    }
    const int h0 = g * 8 + hp * 2;
    float* o0 = po + ((size_t)h0 * NSPLIT + sp) * DH + dp * 2;
    o0[0] = a00; o0[1] = a01;
    float* o1 = po + ((size_t)(h0 + 1) * NSPLIT + sp) * DH + dp * 2;
    o1[0] = a10; o1[1] = a11;
    if (tid < 16) pml[((size_t)(g * 8 + (tid >> 1)) * NSPLIT + sp) * 2 + (tid & 1)] = ml[tid >> 1][tid & 1];
  }
}

__global__ void __launch_bounds__(256) k3(const u16* wo, const u16* w2, const float* po, const float* pml, const float* h, float* part) {
  __shared__ float xs[256];
  __shared__ float wgt[2][NSPLIT];
  __shared__ float acc_s[16][129];
  const int r = blockIdx.x >> 3, c = blockIdx.x & 7, tid = threadIdx.x;
  const u16* wb;
  if (r < 16) {
    wb = wo + (size_t)r * 256 * D;
    if (tid < 128) {
      const int hh = tid >> 6, lane = tid & 63, head = r * 2 + hh;
      float m = pml[((size_t)head * NSPLIT + lane) * 2], l = pml[((size_t)head * NSPLIT + lane) * 2 + 1];
      float M = m;
      for (int o = 32; o > 0; o >>= 1) M = fmaxf(M, __shfl_xor(M, o));
      float w = __expf(m - M);
      float L = w * l;
      for (int o = 32; o > 0; o >>= 1) L += __shfl_xor(L, o);
      wgt[hh][lane] = w / L;
    }
    __syncthreads();
    const int hh = tid >> 7, d = tid & 127, head = r * 2 + hh;
    float a = 0.f;
    for (int s = 0; s < NSPLIT; ++s) a += wgt[hh][s] * po[((size_t)head * NSPLIT + s) * DH + d];
    xs[tid] = a;
  } else {
    wb = w2 + (size_t)(r - 16) * 256 * D;
    xs[tid] = h[(r - 16) * 256 + tid];
  }
  __syncthreads();
  const int cc = (tid & 15) * 8, rr = tid >> 4;
  float a[8] = {0, 0, 0, 0, 0, 0, 0, 0};
#pragma unroll
  for (int p = 0; p < 16; ++p) {
    const int row = p * 16 + rr;
    v4u wv = __builtin_nontemporal_load((const v4u*)(wb + (size_t)row * D + c * 128 + cc));
    const float xv = xs[row];
    a[0] += xv * lo(wv.x); a[1] += xv * hi(wv.x); a[2] += xv * lo(wv.y); a[3] += xv * hi(wv.y);
    a[4] += xv * lo(wv.z); a[5] += xv * hi(wv.z); a[6] += xv * lo(wv.w); a[7] += xv * hi(wv.w);
  }
  for (int i = 0; i < 8; ++i) acc_s[rr][cc + i] = a[i];
  __syncthreads();
  if (tid < 128) {
    float t = 0.f;
    for (int i = 0; i < 16; ++i) t += acc_s[i][tid];
    part[(size_t)r * D + c * 128 + tid] = t;
  }
}

__global__ void __launch_bounds__(256) k4(const u16* x, const u16* b2, const float* part, __hip_bfloat16* out) {
  const int c = blockIdx.x * 256 + threadIdx.x;
  float a = 0.f, b = 0.f;
  for (int r = 0; r < 16; ++r) a += part[(size_t)r * D + c];
  for (int r = 16; r < 30; ++r) b += part[(size_t)r * D + c];
  out[c] = __float2bfloat16(bf2f(x[c]) + a + (b + bf2f(b2[c])));
}

extern "C" void kernel_launch(void* const* d_in, const int* in_sizes, int n_in,
                              void* d_out, int out_size, void* d_ws, size_t ws_size,
                              hipStream_t stream) {
  const u16* x = (const u16*)d_in[0];
  const u16* kc = (const u16*)d_in[1];
  const u16* vc = (const u16*)d_in[2];
  const u16* wq = (const u16*)d_in[3];
  const u16* wo = (const u16*)d_in[4];
  const u16* w1 = (const u16*)d_in[6];
  const u16* b1 = (const u16*)d_in[7];
  const u16* w2 = (const u16*)d_in[8];
  const u16* b2 = (const u16*)d_in[9];
  float* ws = (float*)d_ws;
  float* q = ws;
  float* h = ws + 4096;
  float* pml = ws + 8192;
  float* part = ws + 12288;
  float* po = ws + 65536;
  k1<<<128 + F / 32, 256, 0, stream>>>(x, wq, w1, b1, q, h);
  k2<<<HKV * NSPLIT, 256, 0, stream>>>(kc, vc, q, po, pml);
  k3<<<30 * 8, 256, 0, stream>>>(wo, w2, po, pml, h, part);
  k4<<<4, 256, 0, stream>>>(x, b2, part, (__hip_bfloat16*)d_out);
}
```

```cpp
#include <hip/hip_runtime.h>
#include <hip/hip_bf16.h>
#include <stdint.h>

typedef unsigned v4u __attribute__((ext_vector_type(4)));
typedef unsigned v2u __attribute__((ext_vector_type(2)));
typedef float f32x4 __attribute__((ext_vector_type(4)));
typedef float f32x2 __attribute__((ext_vector_type(2)));
typedef short s16x4 __attribute__((ext_vector_type(4)));
typedef __bf16 bf16x8 __attribute__((ext_vector_type(8)));
typedef __bf16 bf16x2 __attribute__((ext_vector_type(2)));
typedef unsigned short u16;
#define LAS __attribute__((address_space(3)))
#define GAS __attribute__((address_space(1)))
typedef GAS unsigned long long gu64;
#define RLX_AGENT __ATOMIC_RELAXED, __HIP_MEMORY_SCOPE_AGENT
#define RLX_WG __ATOMIC_RELAXED, __HIP_MEMORY_SCOPE_WORKGROUP
#define SB() __builtin_amdgcn_sched_barrier(0)
#define CB() asm volatile("" ::: "memory")
#define PIN() do { CB(); SB(); } while (0)
#define LGKM0() asm volatile("s_waitcnt lgkmcnt(0)" ::: "memory")

#define DEPTH 3
#define ORDER 0
#define TRIG_Q 5
#define TRIG_O 12
#define TRIG_H 15
#define DKV 6
#define HS 8
#define STARTK 1
#define OD 3
#define RSLP 6
#define BPRIO 3
#define APRIO 2
#define FIN_ATOMIC 1

constexpr int DM = 1024, SK = 65536, WIN = 4096, DH = 128, FF = 3584;
constexpr int NWG = 256, NTHR = 1024;
constexpr int NSP = 32, KPS = WIN / NSP;
constexpr int WS_QG = 4096;
constexpr int WS_HG = WS_QG + 4 * 4096 * 8;
constexpr int WS_ML = WS_HG + 4 * FF * 8;
constexpr int WS_OG = WS_ML + 8 * NSP * 128;
constexpr int WS_FG = WS_OG + 32 * NSP * 128 * 8;
constexpr int WS_END = WS_FG + 256 * 64 * 4 * 8;
constexpr int F_QRED = 0, F_Q = 8, F_P = 16, F_K = 24, F_V = 32, F_TQ = 40, F_TO = 48, F_TH = 56, F_HRED = 64, F_XO = 72, F_XH = 80,
              F_FX = 88  , F_FRED = 96, F_W = 104, F_QI = 160, R_LN1 = 112, R_LN2 = 128, R_EP = 144;
constexpr int L_MX = 256;
constexpr int L_REDA = 384;
constexpr int L_REDB = L_REDA + 5120;
constexpr int L_FX = L_REDB + 5120;
constexpr int L_Q = L_FX + 1024;
constexpr int L_P = L_Q + 1088;
constexpr int L_XO = L_P + 1088;
constexpr int L_XH = L_XO + 256;
constexpr int L_W = L_XH + 256;
constexpr int L_XN = L_W + 128;
constexpr int L_K = L_XN + 4096;
constexpr int L_V = L_K + 32768;
constexpr int L_END = L_V + 32768;
constexpr int LDS_BYTES = 96 * 1024;
static_assert(L_END + 2048 <= LDS_BYTES && (L_K % 16) == 0 && (L_Q % 16) == 0 && (L_FX % 16) == 0 && (L_XO % 16) == 0, "lds");
constexpr unsigned MX_EMPTY = 0x7F7FFFFFu;
constexpr unsigned SPIN_CAP = 1u << 15;

#define STAMP(i) do { } while (0)
#define CNT(i, v) do { } while (0)

__device__ __forceinline__ float asf(unsigned u) { return __builtin_bit_cast(float, u); }
__device__ __forceinline__ unsigned asu(float f) { return __builtin_bit_cast(unsigned, f); }
__device__ __forceinline__ float blo(unsigned w) { return asf(w << 16); }
__device__ __forceinline__ float bhi(unsigned w) { return asf(w & 0xffff0000u); }
__device__ __forceinline__ unsigned bfbits(float f) { return (unsigned)__builtin_bit_cast(u16, (__bf16)f); }
__device__ __forceinline__ float rbf(float f) { return asf(bfbits(f) << 16); }
__device__ __forceinline__ unsigned pkbf(f32x2 v) { return __builtin_bit_cast(unsigned, __builtin_convertvector(v, bf16x2)); }
__device__ __forceinline__ f32x2 unpk(unsigned w) { return f32x2{blo(w), bhi(w)}; }
__device__ __forceinline__ float dot2(unsigned a, unsigned b, float c) {
  return __builtin_amdgcn_fdot2_f32_bf16(__builtin_bit_cast(bf16x2, a), __builtin_bit_cast(bf16x2, b), c, false);
}
__device__ __forceinline__ v4u ldnt(const void* p) { return __builtin_nontemporal_load((const GAS v4u*)p); }
__device__ __forceinline__ f32x4 mfma(v4u a, v4u b, f32x4 c) {
  return __builtin_amdgcn_mfma_f32_16x16x32_bf16(__builtin_bit_cast(bf16x8, a), __builtin_bit_cast(bf16x8, b), c, 0, 0, 0);
}
template <int CTRL> __device__ __forceinline__ float dppz(float x) { return asf(__builtin_amdgcn_update_dpp(0, asu(x), CTRL, 0xf, 0xf, true)); }
template <int CTRL> __device__ __forceinline__ float dppm(float x) { return asf(__builtin_amdgcn_mov_dpp(asu(x), CTRL, 0xf, 0xf, true)); }
__device__ __forceinline__ float wave_sum(float v) {
  v += dppz<0x111>(v); v += dppz<0x112>(v); v += dppz<0x114>(v); v += dppz<0x118>(v);
  v += asf(__builtin_amdgcn_update_dpp(0, asu(v), 0x142, 0xa, 0xf, false));
  v += asf(__builtin_amdgcn_update_dpp(0, asu(v), 0x143, 0xc, 0xf, false));
  return asf(__builtin_amdgcn_readlane(asu(v), 63));
}
__device__ __forceinline__ float x16_sum(float x) {
  auto s = __builtin_amdgcn_permlane16_swap(asu(x), asu(x), false, false);
  x = asf(s[0]) + asf(s[1]);
  auto t = __builtin_amdgcn_permlane32_swap(asu(x), asu(x), false, false);
  return asf(t[0]) + asf(t[1]);
}
__device__ __forceinline__ float x16_max(float x) {
  auto s = __builtin_amdgcn_permlane16_swap(asu(x), asu(x), false, false);
  x = fmaxf(asf(s[0]), asf(s[1]));
  auto t = __builtin_amdgcn_permlane32_swap(asu(x), asu(x), false, false);
  return fmaxf(asf(t[0]), asf(t[1]));
}
constexpr int XOR1 = 0xB1, XOR2 = 0x4E, ROR4 = 0x124, ROR8 = 0x128;
__device__ __forceinline__ float par_sum(float v) {
  v += dppm<XOR2>(v); v += dppm<ROR4>(v); v += dppm<ROR8>(v); return x16_sum(v);
}
__device__ __forceinline__ float par_max(float v) {
  v = fmaxf(v, dppm<XOR2>(v)); v = fmaxf(v, dppm<ROR4>(v)); v = fmaxf(v, dppm<ROR8>(v)); return x16_max(v);
}
__device__ __forceinline__ float oct_sum(float v) {
  v += dppm<XOR1>(v); v += dppm<XOR2>(v); v += dppm<0x141>(v); return v;
}
__device__ __forceinline__ float vmax3(float a, float b, float c) { float r; asm("v_max3_f32 %0, %1, %2, %3" : "=v"(r) : "v"(a), "v"(b), "v"(c)); return r; }
__device__ __forceinline__ float x16_max_a(float x) {
  float t;
  asm("v_mov_b32 %1, %0\n\ts_nop 1\n\tv_permlane16_swap_b32 %0, %1\n\tv_max_f32 %0, %0, %1\n\tv_mov_b32 %1, %0\n\ts_nop 1\n\t"
      "v_permlane32_swap_b32 %0, %1\n\tv_max_f32 %0, %0, %1" : "+v"(x), "=&v"(t));
  return x;
}
__device__ __forceinline__ float oct_max_a(float x) {
  asm("s_nop 1\n\tv_max_f32_dpp %0, %0, %0 quad_perm:[1,0,3,2] row_mask:0xf bank_mask:0xf\n\ts_nop 1\n\t"
      "v_max_f32_dpp %0, %0, %0 quad_perm:[2,3,0,1] row_mask:0xf bank_mask:0xf\n\ts_nop 1\n\t"
      "v_max_f32_dpp %0, %0, %0 row_half_mirror row_mask:0xf bank_mask:0xf" : "+v"(x));
  return x;
}
struct Tag { unsigned th, tb; };
__device__ __forceinline__ unsigned glo(float v, Tag t) { return __builtin_amdgcn_alignbit(t.tb, asu(v) + 0x80u, 8); }
__device__ __forceinline__ unsigned gbad_hi(unsigned a, unsigned b, Tag t) { return (a ^ t.th) | (b ^ t.th); }
__device__ __forceinline__ unsigned gbad_lo(unsigned a, unsigned b, Tag t) { return (a ^ (t.tb << 24)) | (b ^ (t.tb << 24)); }
__device__ __forceinline__ float gval(unsigned lo) { return asf(lo << 8); }
__device__ __forceinline__ v4u gld4(__amdgpu_buffer_rsrc_t rs, int off) { return __builtin_amdgcn_raw_buffer_load_b128(rs, off, 0, 16); }
__device__ __forceinline__ v2u gld2(__amdgpu_buffer_rsrc_t rs, int off) { return __builtin_amdgcn_raw_buffer_load_b64(rs, off, 0, 16); }
__device__ __forceinline__ void gst4(__amdgpu_buffer_rsrc_t rs, int off, float a, float b, Tag t) {
  v4u v = {glo(a, t), t.th, glo(b, t), t.th}; __builtin_amdgcn_raw_buffer_store_b128(v, rs, off, 0, 16); }

#define POLL_HEAD "s_mov_b32 %[c], 0x200000\n1:\n\t"
#define POLL_TAIL(SLP) "s_cbranch_vccz 2f\n\t" SLP "s_sub_u32 %[c], %[c], 1\n\ts_cbranch_scc0 1b\n2:\n\ts_waitcnt lgkmcnt(0)"
template <int FO, int SLP> __device__ __forceinline__ void poll8(unsigned z) {
  unsigned long long f; unsigned c;
  asm volatile(POLL_HEAD "ds_read_b64 %[f], %[z] offset:%[fo]\n\ts_waitcnt lgkmcnt(0)\n\tv_cmp_ne_u64 vcc, %[f], %[o]\n\t" POLL_TAIL("s_sleep %[s]\n\t")
               : [f] "=&v"(f), [c] "=&s"(c) : [z] "v"(z), [fo] "n"(FO), [o] "s"(0x0101010101010101ull), [s] "n"(SLP) : "vcc", "scc", "memory");
}
template <int FO, int SLP> __device__ __forceinline__ void poll4(unsigned z) {
  unsigned f, c;
  asm volatile(POLL_HEAD "ds_read_b32 %[f], %[z] offset:%[fo]\n\ts_waitcnt lgkmcnt(0)\n\tv_cmp_ne_u32 vcc, %[o], %[f]\n\t" POLL_TAIL("s_sleep %[s]\n\t")
               : [f] "=&v"(f), [c] "=&s"(c) : [z] "v"(z), [fo] "n"(FO), [o] "s"(0x01010101u), [s] "n"(SLP) : "vcc", "scc", "memory");
}
#define RD4 "ds_read_b128 %[d0], %[a] offset:%[o0]\n\tds_read_b128 %[d1], %[a] offset:%[o1]\n\tds_read_b128 %[d2], %[a] offset:%[o2]\n\tds_read_b128 %[d3], %[a] offset:%[o3]\n\t"
template <int FO, int DO, int ST> __device__ __forceinline__ void poll8_read4(unsigned z, unsigned da, v4u (&d)[4]) {
  unsigned long long f; unsigned c;
  asm volatile("ds_read_b64 %[f], %[z] offset:%[fo]\n\t" RD4 "s_waitcnt lgkmcnt(4)\n\tv_cmp_ne_u64 vcc, %[f], %[o]\n\ts_cbranch_vccz 3f\n\t"
               POLL_HEAD "s_sleep 0\n\tds_read_b64 %[f], %[z] offset:%[fo]\n\ts_waitcnt lgkmcnt(0)\n\tv_cmp_ne_u64 vcc, %[f], %[o]\n\t"
               "s_cbranch_vccz 2f\n\ts_sub_u32 %[c], %[c], 1\n\ts_cbranch_scc0 1b\n2:\n\t" RD4 "3:\n\ts_waitcnt lgkmcnt(0)"
               : [f] "=&v"(f), [c] "=&s"(c), [d0] "=&v"(d[0]), [d1] "=&v"(d[1]), [d2] "=&v"(d[2]), [d3] "=&v"(d[3])
               : [z] "v"(z), [a] "v"(da), [fo] "n"(FO), [o0] "n"(DO), [o1] "n"(DO + ST), [o2] "n"(DO + 2 * ST), [o3] "n"(DO + 3 * ST),
                 [o] "s"(0x0101010101010101ull) : "vcc", "scc", "memory");
}
template <int DO, int ST, int SLP> __device__ __forceinline__ void poll4_read4(unsigned fa, unsigned da, v4u (&d)[4]) {
  unsigned f, c;
  asm volatile("ds_read_b32 %[f], %[fa]\n\t" RD4 "s_waitcnt lgkmcnt(4)\n\tv_cmp_ne_u32 vcc, %[o], %[f]\n\ts_cbranch_vccz 3f\n\t"
               POLL_HEAD "s_sleep %[s]\n\tds_read_b32 %[f], %[fa]\n\ts_waitcnt lgkmcnt(0)\n\tv_cmp_ne_u32 vcc, %[o], %[f]\n\t"
               "s_cbranch_vccz 2f\n\ts_sub_u32 %[c], %[c], 1\n\ts_cbranch_scc0 1b\n2:\n\t" RD4 "3:\n\ts_waitcnt lgkmcnt(0)"
               : [f] "=&v"(f), [c] "=&s"(c), [d0] "=&v"(d[0]), [d1] "=&v"(d[1]), [d2] "=&v"(d[2]), [d3] "=&v"(d[3])
               : [fa] "v"(fa), [a] "v"(da), [o0] "n"(DO), [o1] "n"(DO + ST), [o2] "n"(DO + 2 * ST), [o3] "n"(DO + 3 * ST),
                 [o] "s"(0x01010101u), [s] "n"(SLP) : "vcc", "scc", "memory");
}
template <int SLP> __device__ __forceinline__ v4u poll1_read1(unsigned fa, unsigned da) {
  unsigned f, c; v4u d;
  asm volatile(POLL_HEAD "ds_read_u8 %[f], %[fa]\n\tds_read_b128 %[d], %[a]\n\ts_waitcnt lgkmcnt(1)\n\tv_cmp_eq_u32 vcc, 0, %[f]\n\t" POLL_TAIL("s_sleep %[s]\n\t")
               : [f] "=&v"(f), [c] "=&s"(c), [d] "=&v"(d) : [fa] "v"(fa), [a] "v"(da), [s] "n"(SLP) : "vcc", "scc", "memory");
  return d;
}
template <int NB, int DO, int SLP> __device__ __forceinline__ void pollb_read4w(unsigned fa, unsigned da, unsigned (&d)[4]) {
  unsigned f, c;
#define RD4W "ds_read_b32 %[d0], %[a] offset:%[o0]\n\tds_read_b32 %[d1], %[a] offset:%[o1]\n\tds_read_b32 %[d2], %[a] offset:%[o2]\n\tds_read_b32 %[d3], %[a] offset:%[o3]\n\t"
#define POLLB(RD) asm volatile(RD " %[f], %[fa]\n\t" RD4W "s_waitcnt lgkmcnt(4)\n\tv_cmp_ne_u32 vcc, %[o], %[f]\n\ts_cbranch_vccz 3f\n\t" \
               POLL_HEAD "s_sleep %[s]\n\t" RD " %[f], %[fa]\n\ts_waitcnt lgkmcnt(0)\n\tv_cmp_ne_u32 vcc, %[o], %[f]\n\t" \
               "s_cbranch_vccz 2f\n\ts_sub_u32 %[c], %[c], 1\n\ts_cbranch_scc0 1b\n2:\n\t" RD4W "3:\n\ts_waitcnt lgkmcnt(0)" \
               : [f] "=&v"(f), [c] "=&s"(c), [d0] "=&v"(d[0]), [d1] "=&v"(d[1]), [d2] "=&v"(d[2]), [d3] "=&v"(d[3]) \
               : [fa] "v"(fa), [a] "v"(da), [o0] "n"(DO), [o1] "n"(DO + 32), [o2] "n"(DO + 64), [o3] "n"(DO + 96), [o] "s"(NB == 1 ? 1u : 0x0101u), [s] "n"(SLP) \
               : "vcc", "scc", "memory")
  if (NB == 1) POLLB("ds_read_u8"); else POLLB("ds_read_u16");
}
template <int FO, int DO, int SLP> __device__ __forceinline__ void pollw_read4h(unsigned z, unsigned da, unsigned (&d)[4]) {
  unsigned f, c;
#define RD4H "ds_read_u16 %[d0], %[a] offset:%[o0]\n\tds_read_u16 %[d1], %[a] offset:%[o1]\n\tds_read_u16 %[d2], %[a] offset:%[o2]\n\tds_read_u16 %[d3], %[a] offset:%[o3]\n\t"
  asm volatile(POLL_HEAD "ds_read_b32 %[f], %[z] offset:%[fo]\n\t" RD4H "s_waitcnt lgkmcnt(4)\n\tv_cmp_eq_u32 vcc, 0, %[f]\n\t" POLL_TAIL("s_sleep %[s]\n\t")
               : [f] "=&v"(f), [c] "=&s"(c), [d0] "=&v"(d[0]), [d1] "=&v"(d[1]), [d2] "=&v"(d[2]), [d3] "=&v"(d[3])
               : [z] "v"(z), [a] "v"(da), [fo] "n"(FO), [o0] "n"(DO), [o1] "n"(DO + 16), [o2] "n"(DO + 32), [o3] "n"(DO + 48), [s] "n"(SLP) : "vcc", "scc", "memory");
}
template <int RO, int SLP> __device__ __forceinline__ v4u poll_rec(unsigned z) {
  unsigned f, c; v4u d;
  asm volatile(POLL_HEAD "ds_read_b32 %[f], %[z] offset:%[ro]\n\tds_read_b128 %[d], %[z] offset:%[ro]\n\ts_waitcnt lgkmcnt(1)\n\tv_cmp_eq_u32 vcc, 0, %[f]\n\t" POLL_TAIL("s_sleep %[s]\n\t")
               : [f] "=&v"(f), [d] "=&v"(d), [c] "=&s"(c) : [z] "v"(z), [ro] "n"(RO), [s] "n"(SLP) : "vcc", "scc", "memory");
  return d;
}
__device__ __forceinline__ void flag_at(unsigned addr, unsigned one) {
  asm volatile("s_mov_b64 exec, 1\n\tds_write_b8 %0, %1\n\ts_mov_b64 exec, -1" :: "v"(addr), "v"(one) : "memory");
}
template <int OFF> __device__ __forceinline__ void flag_off(unsigned addr, unsigned one) {
  asm volatile("s_mov_b64 exec, 1\n\tds_write_b8 %0, %1 offset:%2\n\ts_mov_b64 exec, -1" :: "v"(addr), "v"(one), "n"(OFF) : "memory");
}
#define FLAG(F) flag_off<F>(lzw, one)
#define LANDED(v) do { asm volatile("" : "+v"(v)); SB(); } while (0)

__device__ __forceinline__ v4u sel_operand(unsigned xb, int n) {
  const unsigned sh = xb << (16 * (n & 1)); const int k = (n & 7) >> 1;
  return v4u{k == 0 ? sh : 0u, k == 1 ? sh : 0u, k == 2 ? sh : 0u, k == 3 ? sh : 0u};
}
struct LnStat { float mean, rinv; };
__device__ __forceinline__ LnStat ln_stat(const unsigned (&u)[8]) {
  float s0 = 0.f, s1 = 0.f;
#pragma unroll
  for (int i = 0; i < 8; i += 2) { s0 = dot2(u[i], 0x3F803F80u, s0); s1 = dot2(u[i + 1], 0x3F803F80u, s1); }
  const float mean = wave_sum(s0 + s1) * (1.f / DM);
  f32x2 q0 = {0.f, 0.f}, q1 = {0.f, 0.f};
#pragma unroll
  for (int i = 0; i < 8; i += 2) { const f32x2 d0 = unpk(u[i]) - mean, d1 = unpk(u[i + 1]) - mean; q0 += d0 * d0; q1 += d1 * d1; }
  q0 += q1;
  const float var = wave_sum(q0[0] + q0[1]) * (1.f / DM);
  const float sd = rbf(__builtin_amdgcn_sqrtf(rbf(rbf(var) + 1e-5f)));
  return LnStat{rbf(mean), __builtin_amdgcn_rcpf(sd)};
}
__device__ __forceinline__ float ln_apply(float x, LnStat s) { return rbf(rbf(x - s.mean) * s.rinv); }
__device__ __forceinline__ int swz(int row, int ch) { return 256 * row + 16 * (ch ^ (((row & 3) << 2) | ((row >> 2) & 3))); }
__device__ __forceinline__ float sum16(const v4u (&d)[4]) {
  const f32x4 e = (__builtin_bit_cast(f32x4, d[0]) + __builtin_bit_cast(f32x4, d[1])) + (__builtin_bit_cast(f32x4, d[2]) + __builtin_bit_cast(f32x4, d[3]));
  return (e[0] + e[1]) + (e[2] + e[3]);
}
__host__ __device__ constexpr int slot_kind(int i) {
  return i >= 16 ? 4 : i >= 12 ? 3 : ORDER == 0 ? i / 4 : ORDER == 1 ? (i < 4 ? 0 : i < 8 ? 2 : 1) : (i < 4 ? 2 : i < 8 ? 0 : 1);
}
__host__ __device__ constexpr int top_slot(int c) {
  const int a = c + DKV < 7 ? c + DKV : 7, d = c + DEPTH < 19 ? c + DEPTH : 19, t = a > d ? a : d;
  return (HS > 0 && c < TRIG_Q && t >= HS) ? HS - 1 : t;
}
constexpr int W1_FIRST = ORDER == 0 ? 8 : ORDER == 1 ? 4 : 0;

__global__ void __launch_bounds__(NTHR) fused_kernel(const u16* x, const u16* kc, const u16* vc, const u16* wq, const u16* wo,
                                                      const u16* w1, const u16* b1, const u16* w2, const u16* b2,
                                                      u16* out, unsigned char* ws) {
  __shared__ __attribute__((aligned(16))) unsigned char lds_raw[LDS_BYTES];
  LAS unsigned char* L = (LAS unsigned char*)lds_raw;
  const int tid = threadIdx.x, lane = tid & 63, wave = __builtin_amdgcn_readfirstlane(tid >> 6);
  const int b = blockIdx.x, g4 = lane >> 4, n = lane & 15, rl = lane >> 3;
  v4u xa0, xa1; unsigned long long ep;
  if (wave == 0) {
    xa0 = *(const GAS v4u*)((const char*)x + (unsigned)(lane * 32)); xa1 = *(const GAS v4u*)((const char*)x + (unsigned)(lane * 32) + 16);
  }
  PIN();
  const int cgq = b & 63, rgq = b >> 6;
  const bool has1 = b < 224;
  const int cg1 = (b + 4) % 56, rg1 = has1 ? b / 56 : 0;
  const int cg = (b >> 3) & 3, rg = (b & 7) + 8 * (b >> 5);
  const int hh = (b >> 3) & 1, tkv = (b & 7) + 8 * (b >> 4), g = tkv >> 5, sp = tkv & 31;
  unsigned lz = (unsigned)(size_t)L;
  const bool isA = wave >= 1 && wave <= 8;
  const int w = isA ? wave - 1 : (wave == 0 ? 0 : wave - 8);
  unsigned lzw = lz + w, one = 1u;
  asm volatile("" : "+v"(lz), "+v"(lzw), "+v"(one));
  const int redw = (32 * (g4 & 1) + (n & 7)) * 80 + (g4 >> 1) * 4;

  if (isA) {
    const char* wqb = (const char*)(wq + (size_t)(rgq * 256 + w * 32) * 4096 + cgq * 64);
    const unsigned wql = rl * 8192 + (lane & 7) * 16;
    v4u wv[4];
#pragma unroll
    for (int i = 0; i < 4; ++i) wv[i] = ldnt(wqb + i * 65536 + wql);
    PIN();
    if (w == 1) ep = __hip_atomic_load((gu64*)ws, RLX_AGENT);
    PIN();
    if (w == 7) { ((LAS unsigned*)L)[lane] = 0u; if (lane < 32) ((LAS unsigned*)(L + L_MX))[lane] = MX_EMPTY; LGKM0(); }
    __builtin_amdgcn_s_barrier();
    PIN();
    __builtin_amdgcn_s_setprio(APRIO);
    const __amdgpu_buffer_rsrc_t rs = __builtin_amdgcn_make_buffer_rsrc(ws, 0, WS_END, 0x00020000);
    v4u rec;
    unsigned xr[4];
    v4u msk = sel_operand(0xFFFFu, n);
    asm volatile("" : "+v"(msk));
    pollw_read4h<R_LN1, L_XN, 2>(lz, lz + (rgq * 256 + w * 32 + rl) * 2, xr);
    STAMP(1);
    v4u bq[4];
#pragma unroll
    for (int i = 0; i < 4; ++i) bq[i] = msk & (xr[i] * 0x10001u);
    asm volatile("" : "+v"(bq[0]), "+v"(bq[1]), "+v"(bq[2]), "+v"(bq[3]));
    f32x4 acc = {0.f, 0.f, 0.f, 0.f};
#pragma unroll
    for (int i = 0; i < 4; ++i) acc = mfma(wv[i], bq[i], acc);
    if ((g4 < 2) == (n < 8)) {
#pragma unroll
      for (int r = 0; r < 4; ++r) *(LAS float*)(L + L_REDA + redw + r * 640 + w * 8) = acc[r];
    }
    FLAG(F_QRED);
    STAMP(2);
    if (w == 1) {
      asm volatile("" : "+v"(ep));
      const unsigned long long tg = ep + 1ull;
      rec = v4u{1u, (unsigned)(tg >> 8), (unsigned)tg & 255u, 0u};
      *(LAS v4u*)(L + R_EP) = rec;
    }
    if (w == 1) {
      const Tag tag = {rec.y, rec.z};
      v4u d[4];
      const int off = WS_QG + (rgq * 4096 + cgq * 64 + lane) * 8;
      poll8_read4<F_QRED, L_REDA, 16>(lz, lz + lane * 80, d);
      const float v = sum16(d), vn = dppm<XOR1>(v);
      if (!(lane & 1)) gst4(rs, off, v, vn, tag);
      STAMP(9);
    }
    if (w != 1) rec = poll_rec<R_EP, 1>(lz);
    const Tag tag = {rec.y, rec.z};
    const int hq = g * 2 + hh, head = rg >> 1;
    int qoff = WS_QG + ((lane >> 5) * 2 * 4096 + g * 1024 + hh * 512 + (w * 32 + (lane & 31)) * 2) * 8;
    unsigned a_qw = lz + L_Q + (w >> 1) * 272 + ((w & 1) * 32 + (lane & 31)) * 4;
    unsigned a_qp = lz + (n & 3) * 272 + 16 * g4;
    unsigned a_mxw = lz + L_MX + (n & 3) * 32 + w * 4, a_mxr = lz + L_MX + (n & 3) * 32;
    unsigned a_pw = lz + L_P + (n & 3) * 272 + (16 * w + 4 * g4) * 2;
    int ost = WS_OG + (((hq * 4 + g4) * NSP + sp) * 128 + 16 * w + n) * 8;
    int mst = WS_ML + (hq * NSP + sp) * 128 + n * 16;
    asm volatile("" : "+v"(qoff), "+v"(a_qw), "+v"(a_qp), "+v"(a_mxw), "+v"(a_mxr), "+v"(a_pw), "+v"(ost), "+v"(mst));
    int goff; unsigned gaux = 0;
    if (w < 4) goff = WS_OG + ((head * NSP + rl) * 128 + (rg & 1) * 64 + 16 * w + 2 * (lane & 7)) * 8;
    else if (w == 4) goff = WS_ML + ((head >> 2) * NSP + (lane & 31)) * 128 + (head & 3) * 16;
    else { const int pr = (lane & 31) < 28 ? (lane & 31) : 27;
           goff = WS_HG + ((lane >> 5) * FF + rg * 56 + 2 * pr) * 8;
           gaux = *(const GAS unsigned*)((const char*)b1 + (unsigned)((rg * 56 + 2 * pr) * 2)); }
    asm volatile("" : "+v"(goff));
    poll8<F_TQ, 2>(lz);
    STAMP(3);
    v4u q0 = gld4(rs, qoff), q1 = gld4(rs, qoff + 4096 * 8);
    PIN();
    FLAG(F_QI);
    v4u kf[4];
    poll8<F_K, 1>(lz);
#pragma unroll
    for (int s = 0; s < 4; ++s) kf[s] = *(const LAS v4u*)(L + L_K + swz(16 * w + n, 4 * s + g4));
    asm volatile("" : "+v"(kf[0]), "+v"(kf[1]), "+v"(kf[2]), "+v"(kf[3]));
    v2u vt[8];
    unsigned a_vt[2];
#pragma unroll
    for (int tt = 0; tt < 2; ++tt) a_vt[tt] = L_V + swz(8 * g4 + 4 * tt + (n >> 2), 2 * w + ((n & 3) >> 1)) + 8 * (n & 1);
    asm volatile("" : "+v"(a_vt[0]), "+v"(a_vt[1]));
    poll8<F_V, 1>(lz);
#pragma unroll
    for (int ks = 0; ks < 4; ++ks)
#pragma unroll
      for (int tt = 0; tt < 2; ++tt)
        vt[2 * ks + tt] = __builtin_bit_cast(v2u, __builtin_amdgcn_ds_read_tr16_b64_v4i16((LAS s16x4*)(L + a_vt[tt] + ks * 8192)));
    asm volatile("" : "+v"(vt[0]), "+v"(vt[1]), "+v"(vt[2]), "+v"(vt[3]), "+v"(vt[4]), "+v"(vt[5]), "+v"(vt[6]), "+v"(vt[7]));
    unsigned spq = 0;
    _Pragma("nounroll") for (unsigned spin = 0; spin < SPIN_CAP; ++spin) { spq = spin;
      const unsigned bad = gbad_hi(q0.y, q0.w, tag) | gbad_hi(q1.y, q1.w, tag) | ((gbad_lo(q0.x, q0.z, tag) | gbad_lo(q1.x, q1.z, tag)) >> 24);
      if (!__builtin_amdgcn_ballot_w64(bad != 0)) break;
      __builtin_amdgcn_s_sleep(RSLP);
      CB();
      if (bad) { q0 = gld4(rs, qoff); q1 = gld4(rs, qoff + 4096 * 8); }
    }
    STAMP(4); CNT(10, spq);
    { const f32x2 qv = {gval(q0.x) + gval(q1.x), gval(q0.z) + gval(q1.z)};
      auto t0 = __builtin_amdgcn_permlane32_swap(asu(qv[0]), asu(qv[0]), false, false);
      auto t1 = __builtin_amdgcn_permlane32_swap(asu(qv[1]), asu(qv[1]), false, false);
      *(LAS unsigned*)(size_t)a_qw = pkbf(f32x2{asf(t0[0]), asf(t1[0])} + f32x2{asf(t0[1]), asf(t1[1])}); }
    FLAG(F_Q);
    v4u qf[4];
    poll8_read4<F_Q, L_Q, 64>(lz, a_qp, qf);
    f32x4 sc = {0.f, 0.f, 0.f, 0.f};
#pragma unroll
    for (int s = 0; s < 4; ++s) sc = mfma(kf[s], qf[s], sc);
    const unsigned s01 = pkbf(unpk(pkbf(f32x2{sc[0], sc[1]})) * 0.08838834764831845f), s23 = pkbf(unpk(pkbf(f32x2{sc[2], sc[3]})) * 0.08838834764831845f);
    const f32x2 sa = unpk(s01), sb = unpk(s23);
    { const float tm = x16_max_a(vmax3(sa[0], sa[1], vmax3(sb[0], sb[1], sb[1])));
      if (lane < 4) *(LAS float*)(size_t)a_mxw = tm; }
    CB();
    STAMP(5);
    float M;
    _Pragma("nounroll") for (unsigned spin = 0; spin < (1u << 21); ++spin) {
      CB();
      const f32x4 ma = *(const LAS f32x4*)(size_t)a_mxr, mb = *(const LAS f32x4*)(size_t)(a_mxr + 16);
      M = vmax3(vmax3(ma[0], ma[1], ma[2]), vmax3(ma[3], mb[0], mb[1]), vmax3(mb[2], mb[3], mb[3]));
      if (!__builtin_amdgcn_ballot_w64(asu(M) == MX_EMPTY)) break;
    }
    { const float ml2 = M * -1.4426950408889634f;
      const f32x2 ea = sa * 1.4426950408889634f + ml2, eb = sb * 1.4426950408889634f + ml2;
      const v2u pw = {pkbf(f32x2{__builtin_amdgcn_exp2f(ea[0]), __builtin_amdgcn_exp2f(ea[1])}),
                      pkbf(f32x2{__builtin_amdgcn_exp2f(eb[0]), __builtin_amdgcn_exp2f(eb[1])})};
      if (n < 4) *(LAS v2u*)(size_t)a_pw = pw; }
    FLAG(F_P);
    v4u pf[4];
    poll8_read4<F_P, L_P, 64>(lz, a_qp, pf);
    f32x4 oc = {0.f, 0.f, 0.f, 0.f};
#pragma unroll
    for (int ks = 0; ks < 4; ++ks) oc = mfma(pf[ks], v4u{vt[2 * ks].x, vt[2 * ks].y, vt[2 * ks + 1].x, vt[2 * ks + 1].y}, oc);
    { const float ov = g4 < 2 ? (g4 == 0 ? oc[0] : oc[1]) : (g4 == 2 ? oc[2] : oc[3]);
      __builtin_amdgcn_raw_buffer_store_b64(v2u{glo(ov, tag), tag.th}, rs, ost, 0, 16); }
    if (w == 3) {
      f32x4 lc = {0.f, 0.f, 0.f, 0.f};
#pragma unroll
      for (int ks = 0; ks < 4; ++ks) lc = mfma(v4u{0x3F803F80u, 0x3F803F80u, 0x3F803F80u, 0x3F803F80u}, pf[ks], lc);
      if (lane < 4) gst4(rs, mst, M, lc[0], tag);
    }
    STAMP(6);
    if (w < 4) {
      _Pragma("unroll") for (int i = 0; i < OD; ++i) __builtin_amdgcn_s_sleep(8);
      poll8<F_TO, 2>(lz);
      v4u og[4];
#pragma unroll
      for (int k = 0; k < 4; ++k) og[k] = gld4(rs, goff + 8192 * k);
      _Pragma("nounroll") for (unsigned spin = 0; spin < SPIN_CAP; ++spin) { spq = spin;
        unsigned bad[4];
#pragma unroll
        for (int k = 0; k < 4; ++k) bad[k] = gbad_hi(og[k].y, og[k].w, tag) | (gbad_lo(og[k].x, og[k].z, tag) >> 24);
        if (!__builtin_amdgcn_ballot_w64((bad[0] | bad[1] | bad[2] | bad[3]) != 0)) break;
        __builtin_amdgcn_s_sleep(RSLP);
        CB();
#pragma unroll
        for (int k = 0; k < 4; ++k) if (bad[k]) og[k] = gld4(rs, goff + 8192 * k);
      }
      STAMP(7); CNT(11, spq);
      unsigned wt[4];
      pollb_read4w<1, L_W, 1>(lz + F_W, lz + rl * 4, wt);
      f32x2 o2 = f32x2{gval(og[0].x), gval(og[0].z)} * asf(wt[0]);
#pragma unroll
      for (int k = 1; k < 4; ++k) o2 += f32x2{gval(og[k].x), gval(og[k].z)} * asf(wt[k]);
      o2[0] += dppm<ROR8>(o2[0]); o2[1] += dppm<ROR8>(o2[1]);
      const unsigned ob = pkbf(f32x2{x16_sum(o2[0]), x16_sum(o2[1])});
      if (lane < 8) *(LAS v2u*)(L + L_XO + w * 64 + lane * 8) = v2u{__builtin_amdgcn_perm(ob, ob, 0x01000100u), __builtin_amdgcn_perm(ob, ob, 0x03020302u)};
      FLAG(F_XO);
      STAMP(8);
    } else if (w == 4) {
      _Pragma("unroll") for (int i = 0; i < OD; ++i) __builtin_amdgcn_s_sleep(8);
      poll8<F_TO, 2>(lz);
      v4u ml = gld4(rs, goff);
      _Pragma("nounroll") for (unsigned spin = 0; spin < SPIN_CAP; ++spin) { spq = spin;
        const unsigned bad = gbad_hi(ml.y, ml.w, tag) | (gbad_lo(ml.x, ml.z, tag) >> 24);
        if (!__builtin_amdgcn_ballot_w64(bad != 0)) break;
        __builtin_amdgcn_s_sleep(RSLP);
        CB();
        if (bad) ml = gld4(rs, goff);
      }
      STAMP(7); CNT(11, spq);
      const float m = gval(ml.x);
      float mx = oct_max_a(m);
      mx = vmax3(mx, dppm<ROR8>(mx), mx);
      mx = x16_max_a(mx);
      const float wt = __builtin_amdgcn_exp2f((m - mx) * 1.4426950408889634f);
      float ls = oct_sum(wt * gval(ml.z));
      ls += dppm<ROR8>(ls);
      auto t = __builtin_amdgcn_permlane16_swap(asu(ls), asu(ls), false, false);
      if (lane < 32) ((LAS float*)(L + L_W))[lane] = wt * __builtin_amdgcn_rcpf(asf(t[0]) + asf(t[1]));
      flag_off<F_W>(lz, one);
      STAMP(8);
    } else if (w == 5) {
      poll8<F_TH, 2>(lz);
      v4u h0 = gld4(rs, goff), h1 = gld4(rs, goff + 2 * FF * 8);
      _Pragma("nounroll") for (unsigned spin = 0; spin < SPIN_CAP; ++spin) { spq = spin;
        const unsigned bad = gbad_hi(h0.y, h0.w, tag) | gbad_hi(h1.y, h1.w, tag) | ((gbad_lo(h0.x, h0.z, tag) | gbad_lo(h1.x, h1.z, tag)) >> 24);
        if (!__builtin_amdgcn_ballot_w64(bad != 0)) break;
        __builtin_amdgcn_s_sleep(RSLP);
        CB();
        if (bad) { h0 = gld4(rs, goff); h1 = gld4(rs, goff + 2 * FF * 8); }
      }
      STAMP(7); CNT(11, spq);
      const f32x2 hv = {gval(h0.x) + gval(h1.x), gval(h0.z) + gval(h1.z)};
      auto t0 = __builtin_amdgcn_permlane32_swap(asu(hv[0]), asu(hv[0]), false, false);
      auto t1 = __builtin_amdgcn_permlane32_swap(asu(hv[1]), asu(hv[1]), false, false);
      const f32x2 u2 = unpk(pkbf(unpk(pkbf(f32x2{asf(t0[0]), asf(t1[0])} + f32x2{asf(t0[1]), asf(t1[1])})) + unpk(gaux)));
      const f32x2 z = u2 * (u2 * u2 * (0.044715f * -2.3022081981f) + -2.3022081981f);
      const unsigned hb = pkbf(f32x2{u2[0] * __builtin_amdgcn_rcpf(1.f + __builtin_amdgcn_exp2f(z[0])),
                                     u2[1] * __builtin_amdgcn_rcpf(1.f + __builtin_amdgcn_exp2f(z[1]))});
      const int p = lane & 31;
      const int slot = p < 28 ? (p < 14 ? 2 * p : 2 * p + 4) : (p - 28) * 2 + (p < 30 ? 28 : 56);
      const unsigned hz = p < 28 ? hb : 0u;
      if (lane < 32) *(LAS v2u*)(L + L_XH + slot * 4) = v2u{__builtin_amdgcn_perm(hz, hz, 0x01000100u), __builtin_amdgcn_perm(hz, hz, 0x03020302u)};
      flag_off<F_XH>(lz, one);
      STAMP(8);
    } else if (w == 7) {
    } else {
      const int col = (b >> 6) * 256 + ((b >> 4) & 3) * 64 + ((b >> 3) & 1) * 32 + (b & 7) + 8 * (lane & 3);
      float xb2 = asf((unsigned)((const GAS u16*)x)[col] << 16) + asf((unsigned)((const GAS u16*)b2)[col] << 16);
      GAS u16* op = (GAS u16*)out + col;
      gu64* fp = (gu64*)(ws + WS_FG + b * 2048) + lane;
      asm volatile("" : "+v"(xb2), "+v"(op), "+v"(fp));
      poll8<F_FRED, 1>(lz);
      STAMP(7);
      unsigned long long zero = 0; asm volatile("" : "+v"(zero));
      unsigned long long f[4];
#define FIN_LD(k) __hip_atomic_fetch_or(fp + 64 * (k), zero, RLX_AGENT)
#pragma unroll
      for (int k = 0; k < 4; ++k) f[k] = FIN_LD(k);
      _Pragma("nounroll") for (unsigned spin = 0; spin < SPIN_CAP; ++spin) { spq = spin;
        unsigned bad[4];
#pragma unroll
        for (int k = 0; k < 4; ++k) bad[k] = ((unsigned)(f[k] >> 32) ^ tag.th) | (((unsigned)f[k] ^ (tag.tb << 24)) >> 24);
        if (!__builtin_amdgcn_ballot_w64((bad[0] | bad[1] | bad[2] | bad[3]) != 0)) break;
#pragma unroll
        for (int k = 0; k < 4; ++k) if (bad[k]) f[k] = FIN_LD(k);
      }
      float sm = (gval((unsigned)f[0]) + gval((unsigned)f[1])) + (gval((unsigned)f[2]) + gval((unsigned)f[3]));
      sm += dppm<ROR4>(sm); sm += dppm<ROR8>(sm); sm = x16_sum(sm);
      if (lane < 4) *op = (u16)bfbits(sm + xb2);
      STAMP(9); CNT(11, spq);
      if (b == 0 && lane == 0) __hip_atomic_store((gu64*)ws, ((unsigned long long)tag.th << 8) | tag.tb, RLX_AGENT);
    }
  } else {
    const size_t kvb = (((size_t)g * SK + (SK - WIN) + sp * KPS + 16 * w) * DH) * 2;
    const char* kb = (const char*)kc + kvb; const char* vb = (const char*)vc + kvb;
    const unsigned kvl = lane * 16;
    const int cs = w & 3, hf = w >> 2;
    const char* p2 = (const char*)(w2 + (size_t)(rg * 56 + hf * 28) * DM + cg * 256 + cs * 64);
    const char* po = (const char*)(wo + (size_t)(rg * 64 + hf * 32) * DM + cg * 256 + cs * 64);
    const unsigned l2 = rl * 2048 + (lane & 7) * 16;
    const unsigned l2l = rl < 4 ? l2 + 24 * 2048 : l2;
    const char* p1 = has1 ? (const char*)(w1 + (size_t)(rg1 * 256 + w * 32) * FF + cg1 * 64) : p2;
    const unsigned l1 = has1 ? rl * (FF * 2) + (lane & 7) * 16 : l2;
    const int st1 = has1 ? 8 * FF * 2 : 0;
    PIN();
    __builtin_amdgcn_s_barrier();
    PIN();
    __builtin_amdgcn_s_setprio(BPRIO);
    if (w == 3 && b < 8) { __builtin_amdgcn_fence(__ATOMIC_RELEASE, "agent"); }
    if (w == 0) {
      unsigned u[8] = {xa0.x, xa0.y, xa0.z, xa0.w, xa1.x, xa1.y, xa1.z, xa1.w};
      const LnStat t1 = ln_stat(u);
#pragma unroll
      for (int i = 0; i < 8; ++i) u[i] = pkbf(unpk(pkbf(unpk(u[i]) - t1.mean)) * t1.rinv);
      *(LAS v4u*)(L + L_XN + lane * 32) = v4u{u[0], u[1], u[2], u[3]};
      *(LAS v4u*)(L + L_XN + lane * 32 + 16) = v4u{u[4], u[5], u[6], u[7]};
      flag_off<R_LN1>(lz, one);
      STAMP(1);
      xa0 = v4u{u[0], u[1], u[2], u[3]}; xa1 = v4u{u[4], u[5], u[6], u[7]};
    }
    if (STARTK == 1 && w != 0) { const v4u r_ = poll_rec<R_LN1, 2>(lz); }
    if (STARTK == 2) poll4<F_QRED, 1>(lz);
    if (STARTK == 3) poll8<F_QRED, 1>(lz);
    STAMP(2);
    const __amdgpu_buffer_rsrc_t rs = __builtin_amdgcn_make_buffer_rsrc(ws, 0, WS_END, 0x00020000);
    v4u msk = sel_operand(0xFFFFu, n);
    asm volatile("" : "+v"(msk));
    v4u d[20]; v4u bo[4];
    f32x4 acc = {0.f, 0.f, 0.f, 0.f};
    const unsigned a_kv = lz + L_K + 4096 * w + 256 * g4;
#pragma unroll
    for (int c = -1; c < 20; ++c) {
      if (c >= 0) {
        const int kd = slot_kind(c), j = c & 3;
        if (kd < 2) *(LAS v4u*)(size_t)(a_kv + kd * 32768 + 1024 * j + 16 * (n ^ ((g4 << 2) | j))) = d[c];
        else if (kd == 2) acc = mfma(d[c], bo[j], acc);
        else if (top_slot(c) > top_slot(c - 1) || c == TRIG_O || c == TRIG_H) LANDED(d[c]);
        if (kd == 0 && j == 3) FLAG(F_K);
        if (kd == 1 && j == 3) { FLAG(F_V); STAMP(3); }
        if (c == TRIG_Q) FLAG(F_TQ);
        if (c == TRIG_O) FLAG(F_TO);
        if (c == TRIG_H) FLAG(F_TH);
      }
      PIN();
#pragma unroll
      for (int i = c < 0 ? 0 : top_slot(c - 1) + 1; i <= top_slot(c); ++i) {
        const int kd = slot_kind(i), j = i & 3;
        if (HS > 0 && i == HS) poll8<F_QI, 1>(lz);
        if (kd == 0) d[i] = ldnt(kb + j * 1024 + kvl);
        else if (kd == 1) d[i] = ldnt(vb + j * 1024 + kvl);
        else if (kd == 2) d[i] = ldnt(p1 + j * st1 + l1);
        else if (kd == 3) d[i] = j < 3 ? ldnt(p2 + j * 16384 + l2) : ldnt(p2 + l2l);
        else d[i] = ldnt(po + j * 16384 + l2);
        PIN();
      }
      if (c == -1 && w == 0) {
        __builtin_amdgcn_s_setprio(0);
        unsigned u[8] = {xa0.x, xa0.y, xa0.z, xa0.w, xa1.x, xa1.y, xa1.z, xa1.w};
        const LnStat t2 = ln_stat(u);
#pragma unroll
        for (int i = 0; i < 8; ++i) u[i] = pkbf(unpk(pkbf(unpk(u[i]) - t2.mean)) * t2.rinv);
        *(LAS v4u*)(L + L_XN + 2048 + lane * 32) = v4u{u[0], u[1], u[2], u[3]};
        *(LAS v4u*)(L + L_XN + 2048 + lane * 32 + 16) = v4u{u[4], u[5], u[6], u[7]};
        flag_off<R_LN2>(lz, one);
        __builtin_amdgcn_s_setprio(BPRIO);
      }
      if (c + 1 == W1_FIRST) {
        unsigned yr[4];
        pollw_read4h<R_LN2, L_XN + 2048, 1>(lz, lz + (rg1 * 256 + w * 32 + rl) * 2, yr);
#pragma unroll
        for (int k = 0; k < 4; ++k) bo[k] = msk & (yr[k] * 0x10001u);
        asm volatile("" : "+v"(bo[0]), "+v"(bo[1]), "+v"(bo[2]), "+v"(bo[3]));
      }
      if (c == W1_FIRST + 3) {
        if ((g4 < 2) == (n < 8)) {
#pragma unroll
          for (int r = 0; r < 4; ++r) *(LAS float*)(L + L_REDB + redw + r * 640 + w * 8) = acc[r];
        }
        FLAG(F_HRED);
        STAMP(4);
        if (w == 7 && has1) {
          const v4u rp = poll_rec<R_EP, 1>(lz);
          v4u dd[4];
          poll8_read4<F_HRED, L_REDB, 16>(lz, lz + lane * 80, dd);
          const float v = sum16(dd), vn = dppm<XOR1>(v);
          if (!(lane & 1)) gst4(rs, WS_HG + (rg1 * FF + cg1 * 64 + lane) * 8, v, vn, Tag{rp.y, rp.z});
          STAMP(9);
        }
      }
      PIN();
    }
    STAMP(5);
    const v4u rec = poll_rec<R_EP, 1>(lz);
    const Tag tag = {rec.y, rec.z};
    const int slot = (g4 & 1) * 8 + (n & 7);
    unsigned a_fx = lz + L_FX + cs * 256 + slot * 16, a_ff = lz + F_FX + cs, a_x = lz + hf * 128 + rl * 4, a_xf = lz + F_XO + hf * 2;
    int fst = WS_FG + ((cg * 64 + cs * 16 + slot) * 64 + rg) * 32 + (n >> 3) * 16;
    asm volatile("" : "+v"(a_fx), "+v"(a_ff), "+v"(a_x), "+v"(a_xf), "+v"(fst));
    __builtin_amdgcn_s_setprio(1);
    unsigned xm[4];
    acc = f32x4{0.f, 0.f, 0.f, 0.f};
    pollb_read4w<1, L_XH, 1>(lz + F_XH, a_x, xm);
#pragma unroll
    for (int i = 0; i < 4; ++i) acc = mfma(d[12 + i], msk & xm[i], acc);
    pollb_read4w<2, L_XO, 1>(a_xf, a_x, xm);
    FLAG(F_FRED);
    STAMP(6);
#pragma unroll
    for (int i = 0; i < 4; ++i) acc = mfma(d[16 + i], msk & xm[i], acc);
    f32x4 fo;
#pragma unroll
    for (int r = 0; r < 4; ++r) {
      auto sw = __builtin_amdgcn_permlane32_swap(asu(acc[r]), asu(acc[r]), false, false);
      fo[r] = acc[r] + dppm<ROR8>(asf(sw[1]));
    }
    if (hf == 1) {
      if (g4 < 2 && n < 8) *(LAS f32x4*)(size_t)a_fx = fo;
      flag_at(a_ff, one);
    } else {
      fo += __builtin_bit_cast(f32x4, poll1_read1<0>(a_ff, a_fx));
      const float a2 = dppm<ROR8>(fo[2]), a3 = dppm<ROR8>(fo[3]);
      if (g4 < 2) gst4(rs, fst, n < 8 ? fo[0] : a2, n < 8 ? fo[1] : a3, tag);
    }
    STAMP(7);
  }
}

extern "C" void kernel_launch(void* const* d_in, const int* in_sizes, int n_in, void* d_out, int out_size, void* d_ws, size_t ws_size,
                              hipStream_t stream) {
  fused_kernel<<<NWG, NTHR, 0, stream>>>((const u16*)d_in[0], (const u16*)d_in[1], (const u16*)d_in[2], (const u16*)d_in[3], (const u16*)d_in[4],
                                         (const u16*)d_in[6], (const u16*)d_in[7], (const u16*)d_in[8], (const u16*)d_in[9], (u16*)d_out,
                                         (unsigned char*)d_ws);
}
```

```cpp
#include <hip/hip_runtime.h>
#include <hip/hip_bf16.h>
#include <stdint.h>

typedef unsigned v4u __attribute__((ext_vector_type(4)));
typedef unsigned v2u __attribute__((ext_vector_type(2)));
typedef float f32x4 __attribute__((ext_vector_type(4)));
typedef float f32x2 __attribute__((ext_vector_type(2)));
typedef short s16x4 __attribute__((ext_vector_type(4)));
typedef __bf16 bf16x8 __attribute__((ext_vector_type(8)));
typedef __bf16 bf16x2 __attribute__((ext_vector_type(2)));
typedef unsigned short u16;
#define LAS __attribute__((address_space(3)))
#define GAS __attribute__((address_space(1)))
typedef GAS unsigned long long gu64;
#define RLX_AGENT __ATOMIC_RELAXED, __HIP_MEMORY_SCOPE_AGENT
#define SB() __builtin_amdgcn_sched_barrier(0)
#define CB() asm volatile("" ::: "memory")
#define PIN() do { CB(); SB(); } while (0)
#define LGKM0() asm volatile("s_waitcnt lgkmcnt(0)" ::: "memory")

constexpr int DEPTH = 3;
constexpr int DKV = 4;
constexpr int SD = 4;
constexpr int TRIG_Q = 6;
constexpr int TRIG_O = 12;
constexpr int TRIG_H = 14;
constexpr int OD = 4;
constexpr int MLE = 1;
constexpr int RSLP = 6;

constexpr int DM = 1024, SK = 65536, WIN = 4096, DH = 128, FF = 3584;
constexpr int NWG = 256, NTHR = 1024;
constexpr int NSP = 32, KPS = WIN / NSP;
constexpr int WS_QG = 4096;
constexpr int WS_HG = WS_QG + 4 * 4096 * 4;
constexpr int WS_ML = WS_HG + 4 * FF * 4;
constexpr int WS_OG = WS_ML + 8 * NSP * 128;
constexpr int WS_FG = WS_OG + 32 * NSP * 128 * 4;
constexpr int WS_END = WS_FG + 256 * 64 * 32;
constexpr int F_QRED = 0, F_Q = 8, F_P = 16, F_K = 24, F_V = 32, F_TQ = 40, F_TO = 48, F_TH = 56, F_HRED = 64, F_XO = 72, F_XH = 80,
              F_FX = 88  , F_FRED = 96, F_W = 104, F_QI = 160, R_NEXT = 168, R_LN1 = 112, R_LN2 = 128, R_EP = 144;
constexpr int L_MX = 256;
constexpr int L_REDA = 384;
constexpr int L_REDB = L_REDA + 5120;
constexpr int L_FX = L_REDB + 5120;
constexpr int L_Q = L_FX + 4096;
constexpr int L_P = L_Q + 1088;
constexpr int L_XO = L_P + 1088;
constexpr int L_XH = L_XO + 256;
constexpr int L_W = L_XH + 256;
constexpr int L_XN = L_W + 128;
constexpr int L_K = L_XN + 4096;
constexpr int L_V = L_K + 32768;
constexpr int L_END = L_V + 32768;
constexpr int LDS_BYTES = 96 * 1024;
static_assert(L_END + 2048 <= LDS_BYTES && (L_K % 16) == 0 && (L_Q % 16) == 0 && (L_FX % 16) == 0 && (L_XO % 16) == 0, "lds");
constexpr unsigned MX_EMPTY = 0x7F7FFFFFu;
constexpr unsigned SPIN_CAP = 1u << 15;

__device__ __forceinline__ float asf(unsigned u) { return __builtin_bit_cast(float, u); }
__device__ __forceinline__ unsigned asu(float f) { return __builtin_bit_cast(unsigned, f); }
__device__ __forceinline__ float blo(unsigned w) { return asf(w << 16); }
__device__ __forceinline__ float bhi(unsigned w) { return asf(w & 0xffff0000u); }
__device__ __forceinline__ unsigned bfbits(float f) { return (unsigned)__builtin_bit_cast(u16, (__bf16)f); }
__device__ __forceinline__ float rbf(float f) { return asf(bfbits(f) << 16); }
__device__ __forceinline__ unsigned pkbf(f32x2 v) { return __builtin_bit_cast(unsigned, __builtin_convertvector(v, bf16x2)); }
__device__ __forceinline__ f32x2 unpk(unsigned w) { return f32x2{blo(w), bhi(w)}; }
__device__ __forceinline__ float dot2(unsigned a, unsigned b, float c) {
  return __builtin_amdgcn_fdot2_f32_bf16(__builtin_bit_cast(bf16x2, a), __builtin_bit_cast(bf16x2, b), c, false);
}
__device__ __forceinline__ v4u ldnt(const void* p) { return __builtin_nontemporal_load((const GAS v4u*)p); }
__device__ __forceinline__ f32x4 mfma(v4u a, v4u b, f32x4 c) {
  return __builtin_amdgcn_mfma_f32_16x16x32_bf16(__builtin_bit_cast(bf16x8, a), __builtin_bit_cast(bf16x8, b), c, 0, 0, 0);
}
template <int CTRL> __device__ __forceinline__ float dppz(float x) { return asf(__builtin_amdgcn_update_dpp(0, asu(x), CTRL, 0xf, 0xf, true)); }
template <int CTRL> __device__ __forceinline__ float dppm(float x) { return asf(__builtin_amdgcn_mov_dpp(asu(x), CTRL, 0xf, 0xf, true)); }
__device__ __forceinline__ float wave_sum(float v) {
  v += dppz<0x111>(v); v += dppz<0x112>(v); v += dppz<0x114>(v); v += dppz<0x118>(v);
  v += asf(__builtin_amdgcn_update_dpp(0, asu(v), 0x142, 0xa, 0xf, false));
  v += asf(__builtin_amdgcn_update_dpp(0, asu(v), 0x143, 0xc, 0xf, false));
  return asf(__builtin_amdgcn_readlane(asu(v), 63));
}
__device__ __forceinline__ float x16_sum(float x) {
  auto s = __builtin_amdgcn_permlane16_swap(asu(x), asu(x), false, false);
  x = asf(s[0]) + asf(s[1]);
  auto t = __builtin_amdgcn_permlane32_swap(asu(x), asu(x), false, false);
  return asf(t[0]) + asf(t[1]);
}
constexpr int XOR1 = 0xB1, XOR2 = 0x4E, ROR4 = 0x124, ROR8 = 0x128;
__device__ __forceinline__ float oct_sum(float v) {
  v += dppm<XOR1>(v); v += dppm<XOR2>(v); v += dppm<0x141>(v); return v;
}
__device__ __forceinline__ float vmax3(float a, float b, float c) { float r; asm("v_max3_f32 %0, %1, %2, %3" : "=v"(r) : "v"(a), "v"(b), "v"(c)); return r; }
__device__ __forceinline__ float x16_max_a(float x) {
  float t;
  asm volatile("v_mov_b32 %1, %0\n\ts_nop 1\n\tv_permlane16_swap_b32 %0, %1\n\tv_max_f32 %0, %0, %1\n\tv_mov_b32 %1, %0\n\ts_nop 1\n\t"
      "v_permlane32_swap_b32 %0, %1\n\tv_max_f32 %0, %0, %1" : "+v"(x), "=&v"(t));
  return x;
}
__device__ __forceinline__ float oct_max_a(float x) {
  asm volatile("s_nop 1\n\tv_max_f32_dpp %0, %0, %0 quad_perm:[1,0,3,2] row_mask:0xf bank_mask:0xf\n\ts_nop 1\n\t"
      "v_max_f32_dpp %0, %0, %0 quad_perm:[2,3,0,1] row_mask:0xf bank_mask:0xf\n\ts_nop 1\n\t"
      "v_max_f32_dpp %0, %0, %0 row_half_mirror row_mask:0xf bank_mask:0xf" : "+v"(x));
  return x;
}
struct Tag { unsigned p[4], c[4]; };
__device__ __forceinline__ Tag make_tag(unsigned pa, unsigned pb) {
  pa = __builtin_amdgcn_readfirstlane(pa); pb = __builtin_amdgcn_readfirstlane(pb);
  return Tag{{pa, pa >> 10, pb, pb >> 10}, {pa << 22, (pa >> 10) << 22, pb << 22, (pb >> 10) << 22}};
}
__device__ __forceinline__ unsigned gpk(float v, unsigned piece) { return __builtin_amdgcn_alignbit(piece, asu(v) + 0x200u, 10); }
__device__ __forceinline__ bool ubad(v4u u, const Tag& t) { return ((u.x ^ t.c[0]) | (u.y ^ t.c[1]) | (u.z ^ t.c[2]) | (u.w ^ t.c[3])) > 0x3FFFFFu; }
__device__ __forceinline__ float gval(unsigned d) { return asf(d << 10); }
__device__ __forceinline__ f32x4 uval(v4u u) { return __builtin_bit_cast(f32x4, u << 10); }
__device__ __forceinline__ v4u gld4(__amdgpu_buffer_rsrc_t rs, int off, int soff = 0) { return __builtin_amdgcn_raw_buffer_load_b128(rs, off, soff, 16); }
__device__ __forceinline__ void gst4(__amdgpu_buffer_rsrc_t rs, int off, float a, float b, float c, float d, const Tag& t) {
  const v4u v = {gpk(a, t.p[0]), gpk(b, t.p[1]), gpk(c, t.p[2]), gpk(d, t.p[3])};
  __builtin_amdgcn_raw_buffer_store_b128(v, rs, off, 0, 16);
}
struct Tag8 { unsigned th, tb; };
__device__ __forceinline__ Tag8 make_tag8(v4u rec) { return Tag8{(rec.w >> 8) | (((rec.z >> 12) & 255u) << 24), rec.w & 255u}; }
__device__ __forceinline__ unsigned g8pk(float v, Tag8 t) { return __builtin_amdgcn_alignbit(t.tb, asu(v) + 0x80u, 8); }
__device__ __forceinline__ void gst_quad(__amdgpu_buffer_rsrc_t rs, int off, float v, const Tag& t, int lane) {
  const float v1 = dppm<0x55>(v), v2 = dppm<0xAA>(v), v3 = dppm<0xFF>(v);
  if (!(lane & 3)) gst4(rs, off, v, v1, v2, v3, t);
}

#define POLL_HEAD "s_mov_b32 %[c], 0x200000\n1:\n\t"
#define POLL_TAIL(SLP) "s_cbranch_vccz 2f\n\t" SLP "s_sub_u32 %[c], %[c], 1\n\ts_cbranch_scc0 1b\n2:\n\ts_waitcnt lgkmcnt(0)"
template <int FO, int SLP> __device__ __forceinline__ void poll8(unsigned z) {
  unsigned long long f; unsigned c;
  asm volatile(POLL_HEAD "ds_read_b64 %[f], %[z] offset:%[fo]\n\ts_waitcnt lgkmcnt(0)\n\tv_cmp_ne_u64 vcc, %[f], %[o]\n\t" POLL_TAIL("s_sleep %[s]\n\t")
               : [f] "=&v"(f), [c] "=&s"(c) : [z] "v"(z), [fo] "n"(FO), [o] "s"(0x0101010101010101ull), [s] "n"(SLP) : "vcc", "scc", "memory");
}
template <int FO, int SLP> __device__ __forceinline__ void poll4(unsigned z) {
  unsigned f, c;
  asm volatile(POLL_HEAD "ds_read_b32 %[f], %[z] offset:%[fo]\n\ts_waitcnt lgkmcnt(0)\n\tv_cmp_ne_u32 vcc, %[o], %[f]\n\t" POLL_TAIL("s_sleep %[s]\n\t")
               : [f] "=&v"(f), [c] "=&s"(c) : [z] "v"(z), [fo] "n"(FO), [o] "s"(0x01010101u), [s] "n"(SLP) : "vcc", "scc", "memory");
}
#define RD4 "ds_read_b128 %[d0], %[a] offset:%[o0]\n\tds_read_b128 %[d1], %[a] offset:%[o1]\n\tds_read_b128 %[d2], %[a] offset:%[o2]\n\tds_read_b128 %[d3], %[a] offset:%[o3]\n\t"
template <int FO, int DO, int ST> __device__ __forceinline__ void poll8_read4(unsigned z, unsigned da, v4u (&d)[4]) {
  unsigned long long f; unsigned c;
  asm volatile("ds_read_b64 %[f], %[z] offset:%[fo]\n\t" RD4 "s_waitcnt lgkmcnt(4)\n\tv_cmp_ne_u64 vcc, %[f], %[o]\n\ts_cbranch_vccz 3f\n\t"
               POLL_HEAD "s_sleep 0\n\tds_read_b64 %[f], %[z] offset:%[fo]\n\ts_waitcnt lgkmcnt(0)\n\tv_cmp_ne_u64 vcc, %[f], %[o]\n\t"
               "s_cbranch_vccz 2f\n\ts_sub_u32 %[c], %[c], 1\n\ts_cbranch_scc0 1b\n2:\n\t" RD4 "3:\n\ts_waitcnt lgkmcnt(0)"
               : [f] "=&v"(f), [c] "=&s"(c), [d0] "=&v"(d[0]), [d1] "=&v"(d[1]), [d2] "=&v"(d[2]), [d3] "=&v"(d[3])
               : [z] "v"(z), [a] "v"(da), [fo] "n"(FO), [o0] "n"(DO), [o1] "n"(DO + ST), [o2] "n"(DO + 2 * ST), [o3] "n"(DO + 3 * ST),
                 [o] "s"(0x0101010101010101ull) : "vcc", "scc", "memory");
}
template <int DO, int ST, int SLP> __device__ __forceinline__ void poll4_read4(unsigned fa, unsigned da, v4u (&d)[4]) {
  unsigned f, c;
  asm volatile("ds_read_b32 %[f], %[fa]\n\t" RD4 "s_waitcnt lgkmcnt(4)\n\tv_cmp_ne_u32 vcc, %[o], %[f]\n\ts_cbranch_vccz 3f\n\t"
               POLL_HEAD "s_sleep %[s]\n\tds_read_b32 %[f], %[fa]\n\ts_waitcnt lgkmcnt(0)\n\tv_cmp_ne_u32 vcc, %[o], %[f]\n\t"
               "s_cbranch_vccz 2f\n\ts_sub_u32 %[c], %[c], 1\n\ts_cbranch_scc0 1b\n2:\n\t" RD4 "3:\n\ts_waitcnt lgkmcnt(0)"
               : [f] "=&v"(f), [c] "=&s"(c), [d0] "=&v"(d[0]), [d1] "=&v"(d[1]), [d2] "=&v"(d[2]), [d3] "=&v"(d[3])
               : [fa] "v"(fa), [a] "v"(da), [o0] "n"(DO), [o1] "n"(DO + ST), [o2] "n"(DO + 2 * ST), [o3] "n"(DO + 3 * ST),
                 [o] "s"(0x01010101u), [s] "n"(SLP) : "vcc", "scc", "memory");
}
template <int SLP> __device__ __forceinline__ v4u poll1_read1(unsigned fa, unsigned da) {
  unsigned f, c; v4u d;
  asm volatile(POLL_HEAD "ds_read_u8 %[f], %[fa]\n\tds_read_b128 %[d], %[a]\n\ts_waitcnt lgkmcnt(1)\n\tv_cmp_eq_u32 vcc, 0, %[f]\n\t" POLL_TAIL("s_sleep %[s]\n\t")
               : [f] "=&v"(f), [c] "=&s"(c), [d] "=&v"(d) : [fa] "v"(fa), [a] "v"(da), [s] "n"(SLP) : "vcc", "scc", "memory");
  return d;
}
template <int NB, int DO, int SLP> __device__ __forceinline__ void pollb_read4w(unsigned fa, unsigned da, unsigned (&d)[4]) {
  unsigned f, c;
#define RD4W "ds_read_b32 %[d0], %[a] offset:%[o0]\n\tds_read_b32 %[d1], %[a] offset:%[o1]\n\tds_read_b32 %[d2], %[a] offset:%[o2]\n\tds_read_b32 %[d3], %[a] offset:%[o3]\n\t"
#define POLLB(RD) asm volatile(RD " %[f], %[fa]\n\t" RD4W "s_waitcnt lgkmcnt(4)\n\tv_cmp_ne_u32 vcc, %[o], %[f]\n\ts_cbranch_vccz 3f\n\t" \
               POLL_HEAD "s_sleep %[s]\n\t" RD " %[f], %[fa]\n\ts_waitcnt lgkmcnt(0)\n\tv_cmp_ne_u32 vcc, %[o], %[f]\n\t" \
               "s_cbranch_vccz 2f\n\ts_sub_u32 %[c], %[c], 1\n\ts_cbranch_scc0 1b\n2:\n\t" RD4W "3:\n\ts_waitcnt lgkmcnt(0)" \
               : [f] "=&v"(f), [c] "=&s"(c), [d0] "=&v"(d[0]), [d1] "=&v"(d[1]), [d2] "=&v"(d[2]), [d3] "=&v"(d[3]) \
               : [fa] "v"(fa), [a] "v"(da), [o0] "n"(DO), [o1] "n"(DO + 32), [o2] "n"(DO + 64), [o3] "n"(DO + 96), [o] "s"(NB == 1 ? 1u : 0x0101u), [s] "n"(SLP) \
               : "vcc", "scc", "memory")
  if (NB == 1) POLLB("ds_read_u8"); else POLLB("ds_read_u16");
}
template <int FO, int DO, int SLP> __device__ __forceinline__ void pollw_read4h(unsigned z, unsigned da, unsigned (&d)[4]) {
  unsigned f, c;
#define RD4H "ds_read_u16 %[d0], %[a] offset:%[o0]\n\tds_read_u16 %[d1], %[a] offset:%[o1]\n\tds_read_u16 %[d2], %[a] offset:%[o2]\n\tds_read_u16 %[d3], %[a] offset:%[o3]\n\t"
  asm volatile(POLL_HEAD "ds_read_b32 %[f], %[z] offset:%[fo]\n\t" RD4H "s_waitcnt lgkmcnt(4)\n\tv_cmp_eq_u32 vcc, 0, %[f]\n\t" POLL_TAIL("s_sleep %[s]\n\t")
               : [f] "=&v"(f), [c] "=&s"(c), [d0] "=&v"(d[0]), [d1] "=&v"(d[1]), [d2] "=&v"(d[2]), [d3] "=&v"(d[3])
               : [z] "v"(z), [a] "v"(da), [fo] "n"(FO), [o0] "n"(DO), [o1] "n"(DO + 16), [o2] "n"(DO + 32), [o3] "n"(DO + 48), [s] "n"(SLP) : "vcc", "scc", "memory");
}
template <int RO, int SLP> __device__ __forceinline__ v4u poll_rec(unsigned z) {
  unsigned f, c; v4u d;
  asm volatile(POLL_HEAD "ds_read_b32 %[f], %[z] offset:%[ro]\n\tds_read_b128 %[d], %[z] offset:%[ro]\n\ts_waitcnt lgkmcnt(1)\n\tv_cmp_eq_u32 vcc, 0, %[f]\n\t" POLL_TAIL("s_sleep %[s]\n\t")
               : [f] "=&v"(f), [d] "=&v"(d), [c] "=&s"(c) : [z] "v"(z), [ro] "n"(RO), [s] "n"(SLP) : "vcc", "scc", "memory");
  return d;
}
__device__ __forceinline__ void flag_at(unsigned addr, unsigned one) {
  asm volatile("s_mov_b64 exec, 1\n\tds_write_b8 %0, %1\n\ts_mov_b64 exec, -1" :: "v"(addr), "v"(one) : "memory");
}
template <int OFF> __device__ __forceinline__ void flag_off(unsigned addr, unsigned one) {
  asm volatile("s_mov_b64 exec, 1\n\tds_write_b8 %0, %1 offset:%2\n\ts_mov_b64 exec, -1" :: "v"(addr), "v"(one), "n"(OFF) : "memory");
}
#define FLAG(F) flag_off<F>(lzw, one)
#define LANDED(v) do { asm volatile("" : "+v"(v)); SB(); } while (0)

__device__ __forceinline__ v4u sel_operand(unsigned xb, int n) {
  const unsigned sh = xb << (16 * (n & 1)); const int k = (n & 7) >> 1;
  return v4u{k == 0 ? sh : 0u, k == 1 ? sh : 0u, k == 2 ? sh : 0u, k == 3 ? sh : 0u};
}
struct LnStat { float mean, rinv; };
__device__ __forceinline__ LnStat ln_stat(const unsigned (&u)[8]) {
  float s0 = 0.f, s1 = 0.f;
#pragma unroll
  for (int i = 0; i < 8; i += 2) { s0 = dot2(u[i], 0x3F803F80u, s0); s1 = dot2(u[i + 1], 0x3F803F80u, s1); }
  const float mean = wave_sum(s0 + s1) * (1.f / DM);
  f32x2 q0 = {0.f, 0.f}, q1 = {0.f, 0.f};
#pragma unroll
  for (int i = 0; i < 8; i += 2) { const f32x2 d0 = unpk(u[i]) - mean, d1 = unpk(u[i + 1]) - mean; q0 += d0 * d0; q1 += d1 * d1; }
  q0 += q1;
  const float var = wave_sum(q0[0] + q0[1]) * (1.f / DM);
  const float sd = rbf(__builtin_amdgcn_sqrtf(rbf(rbf(var) + 1e-5f)));
  return LnStat{rbf(mean), __builtin_amdgcn_rcpf(sd)};
}
__device__ __forceinline__ int swz(int row, int ch) { return 256 * row + 16 * (ch ^ (((row & 3) << 2) | ((row >> 2) & 3))); }
__device__ __forceinline__ float sum16(const v4u (&d)[4]) {
  const f32x4 e = (__builtin_bit_cast(f32x4, d[0]) + __builtin_bit_cast(f32x4, d[1])) + (__builtin_bit_cast(f32x4, d[2]) + __builtin_bit_cast(f32x4, d[3]));
  return (e[0] + e[1]) + (e[2] + e[3]);
}
__host__ __device__ constexpr int slot_kind(int i) { return i >= 16 ? 4 : i >= 12 ? 3 : i < 4 ? 0 : i < 5 ? 2 : i < 9 ? 1 : 2; }
__host__ __device__ constexpr int slot_idx(int i) { return i >= 12 ? (i & 3) : i < 4 ? i : i < 5 ? 0 : i < 9 ? i - 5 : i - 8; }
constexpr int HOLD = 9;
__host__ __device__ constexpr int top_slot(int c) {
  const int d0 = c + (c < TRIG_Q ? DKV : DEPTH), d = d0 < 19 ? d0 : 19;
  return c < TRIG_Q ? (d < HOLD - 1 ? d : HOLD - 1) : (d > HOLD - 1 ? d : HOLD - 1);
}
static_assert(TRIG_Q < HOLD, "the q trigger must not depend on a held slot");
constexpr int W1_FIRST = 4, W1_LAST = 11;

template <int V> struct IC { static constexpr int value = V; };
template <int B, int E, class F> __device__ __forceinline__ void static_for(F&& f) {
  if constexpr (B < E) { f(IC<B>{}); static_for<B + 1, E>(f); }
}

__global__ void __launch_bounds__(NTHR) fused_kernel(const u16* x, const u16* kc, const u16* vc, const u16* wq, const u16* wo,
                                                      const u16* w1, const u16* b1, const u16* w2, const u16* b2,
                                                      u16* out, unsigned char* ws) {
  __shared__ __attribute__((aligned(16))) unsigned char lds_raw[LDS_BYTES];
  LAS unsigned char* L = (LAS unsigned char*)lds_raw;
  const int tid = threadIdx.x, lane = tid & 63, wave = __builtin_amdgcn_readfirstlane(tid >> 6);
  const int b = blockIdx.x, g4 = lane >> 4, n = lane & 15, rl = lane >> 3;
  v4u xa0, xa1; unsigned long long ep;
  if (wave == 0) {
    xa0 = *(const GAS v4u*)((const char*)x + (unsigned)(lane * 32)); xa1 = *(const GAS v4u*)((const char*)x + (unsigned)(lane * 32) + 16);
  }
  PIN();
  if (wave >= 1 && wave <= 8) __builtin_amdgcn_s_setprio(3);
  const int cgq = b & 63, rgq = b >> 6;
  const bool has1 = b < 224;
  const int cg1 = (b + 4) % 56, rg1 = has1 ? b / 56 : 0;
  const int cg = (b >> 3) & 3, rg = (b & 7) + 8 * (b >> 5);
  const int hh = (b >> 3) & 1, tkv = (b & 7) + 8 * (b >> 4), g = tkv >> 5, sp = tkv & 31;
  unsigned lz = (unsigned)(size_t)L;
  const bool isA = wave >= 1 && wave <= 8;
  const int w = isA ? wave - 1 : (wave == 0 ? 0 : wave - 8);
  unsigned lzw = lz + w, one = 1u;
  asm volatile("" : "+v"(lz), "+v"(lzw), "+v"(one));
  const int redw = (32 * (g4 & 1) + (n & 7)) * 80 + (g4 >> 1) * 4;

  if (isA) {
    const char* wqb = (const char*)(wq + (size_t)(rgq * 256 + w * 32) * 4096 + cgq * 64);
    const unsigned wql = rl * 8192 + (lane & 7) * 16;
    v4u wv[4];
#pragma unroll
    for (int i = 0; i < 4; ++i) wv[i] = ldnt(wqb + i * 65536 + wql);
    PIN();
    if (w == 1) ep = __hip_atomic_load((gu64*)ws, RLX_AGENT);
    PIN();
    __builtin_amdgcn_s_barrier();
    PIN();
    __builtin_amdgcn_s_setprio(2);
    const __amdgpu_buffer_rsrc_t rs = __builtin_amdgcn_make_buffer_rsrc(ws, 0, WS_END, 0x00020000);
    v4u rec;
    unsigned xr[4];
    v4u msk = sel_operand(0xFFFFu, n);
    asm volatile("" : "+v"(msk));
    pollw_read4h<R_LN1, L_XN, 2>(lz, lz + (rgq * 256 + w * 32 + rl) * 2, xr);
    v4u bq[4];
#pragma unroll
    for (int i = 0; i < 4; ++i) bq[i] = msk & (xr[i] * 0x10001u);
    asm volatile("" : "+v"(bq[0]), "+v"(bq[1]), "+v"(bq[2]), "+v"(bq[3]));
    f32x4 acc = {0.f, 0.f, 0.f, 0.f};
#pragma unroll
    for (int i = 0; i < 4; ++i) acc = mfma(wv[i], bq[i], acc);
    if ((g4 < 2) == (n < 8)) {
#pragma unroll
      for (int r = 0; r < 4; ++r) *(LAS float*)(L + L_REDA + redw + r * 640 + w * 8) = acc[r];
    }
    FLAG(F_QRED);
    if (w == 1) {
      asm volatile("" : "+v"(ep));
      const unsigned long long tg = ep + 1ull;
      const unsigned lo = (unsigned)tg, hi = (unsigned)(tg >> 20);
      rec = v4u{1u, lo & 0xFFFFFu, (hi & 0xFFC00u) | ((hi + lo) & 0x3FFu), lo};
      *(LAS v2u*)(L + R_NEXT) = v2u{lo, (unsigned)(tg >> 32)};
      CB();
      *(LAS v4u*)(L + R_EP) = rec;
      v4u d[4];
      const int off = WS_QG + (rgq * 4096 + cgq * 64 + lane) * 4;
      const Tag tg1 = make_tag(rec.y, rec.z);
      poll8_read4<F_QRED, L_REDA, 16>(lz, lz + lane * 80, d);
      gst_quad(rs, off, sum16(d), tg1, lane);
    } else {
      rec = poll_rec<R_EP, 1>(lz);
    }
    const Tag tag = make_tag(rec.y, rec.z);
    const int hq = g * 2 + hh, head = rg >> 1;
    int qoff = WS_QG + ((lane >> 5) * 4096 + g * 1024 + hh * 512 + (w & 3) * 128 + (lane & 31) * 4) * 4;
    unsigned a_qw = lz + L_Q + (w & 3) * 272 + (lane & 31) * 8;
    unsigned a_qp = lz + (n & 3) * 272 + 16 * g4;
    unsigned a_mxw = lz + L_MX + (n & 3) * 32 + w * 4, a_mxr = lz + L_MX + (n & 3) * 32;
    unsigned a_pw = lz + L_P + (n & 3) * 272 + (16 * w + 4 * g4) * 2;
    int ost = WS_OG + (((hq * 4 + g4) * NSP + sp) * 128 + 16 * w + n) * 4;
    int mst = WS_ML + (hq * NSP + sp) * 128 + n * 16;
    asm volatile("" : "+v"(qoff), "+v"(a_qw), "+v"(a_qp), "+v"(a_mxw), "+v"(a_mxr), "+v"(a_pw), "+v"(ost), "+v"(mst));
    int goff; v2u gaux = {0u, 0u};
    if (w < 2) goff = WS_OG + ((head * NSP + rl) * 128 + (rg & 1) * 64 + 32 * w + 4 * (lane & 7)) * 4;
    else if (w == 4) goff = WS_ML + ((head >> 2) * NSP + (lane & 31)) * 128 + (head & 3) * 16;
    else { const int un = n < 14 ? n : 13;
           goff = WS_HG + (g4 * FF + rg * 56 + 4 * un) * 4;
           gaux = *(const GAS v2u*)((const char*)b1 + (unsigned)((rg * 56 + 4 * un) * 2)); }
    asm volatile("" : "+v"(goff));
    poll8<F_TQ, 2>(lz);
    v4u q0, q1;
    if (w < 4) { q0 = gld4(rs, qoff); q1 = gld4(rs, qoff, 2 * 4096 * 4); PIN(); FLAG(F_QI); }
    v4u kf[4];
    poll8<F_K, 1>(lz);
#pragma unroll
    for (int s = 0; s < 4; ++s) kf[s] = *(const LAS v4u*)(L + L_K + swz(16 * w + n, 4 * s + g4));
    asm volatile("" : "+v"(kf[0]), "+v"(kf[1]), "+v"(kf[2]), "+v"(kf[3]));
    v2u vt[8];
    unsigned a_vt[2];
#pragma unroll
    for (int tt = 0; tt < 2; ++tt) a_vt[tt] = L_V + swz(8 * g4 + 4 * tt + (n >> 2), 2 * w + ((n & 3) >> 1)) + 8 * (n & 1);
    asm volatile("" : "+v"(a_vt[0]), "+v"(a_vt[1]));
    poll8<F_V, 1>(lz);
#pragma unroll
    for (int ks = 0; ks < 4; ++ks)
#pragma unroll
      for (int tt = 0; tt < 2; ++tt)
        vt[2 * ks + tt] = __builtin_bit_cast(v2u, __builtin_amdgcn_ds_read_tr16_b64_v4i16((LAS s16x4*)(L + a_vt[tt] + ks * 8192)));
    asm volatile("" : "+v"(vt[0]), "+v"(vt[1]), "+v"(vt[2]), "+v"(vt[3]), "+v"(vt[4]), "+v"(vt[5]), "+v"(vt[6]), "+v"(vt[7]));
    if (w < 4) {
      _Pragma("nounroll") for (unsigned spin = 0; spin < SPIN_CAP; ++spin) {
        const bool bad = ubad(q0, tag) | ubad(q1, tag);
        if (!__builtin_amdgcn_ballot_w64(bad)) break;
        __builtin_amdgcn_s_sleep(RSLP);
        CB();
        if (bad) { q0 = gld4(rs, qoff); q1 = gld4(rs, qoff, 2 * 4096 * 4); }
      }
      const f32x4 qv = uval(q0) + uval(q1);
      f32x4 qa, qb;
#pragma unroll
      for (int i = 0; i < 4; ++i) { auto t = __builtin_amdgcn_permlane32_swap(asu(qv[i]), asu(qv[i]), false, false); qa[i] = asf(t[0]); qb[i] = asf(t[1]); }
      qa += qb;
      *(LAS v2u*)(size_t)a_qw = v2u{pkbf(f32x2{qa[0], qa[1]}), pkbf(f32x2{qa[2], qa[3]})};
      FLAG(F_Q);
    }
    v4u qf[4];
    poll4_read4<L_Q, 64, 0>(lz + F_Q, a_qp, qf);
    f32x4 sc = {0.f, 0.f, 0.f, 0.f};
#pragma unroll
    for (int s = 0; s < 4; ++s) sc = mfma(kf[s], qf[s], sc);
    const unsigned s01 = pkbf(unpk(pkbf(f32x2{sc[0], sc[1]})) * 0.08838834764831845f), s23 = pkbf(unpk(pkbf(f32x2{sc[2], sc[3]})) * 0.08838834764831845f);
    const f32x2 sa = unpk(s01), sb = unpk(s23);
    { const float tm = x16_max_a(vmax3(sa[0], sa[1], vmax3(sb[0], sb[1], sb[1])));
      if (lane < 4) *(LAS float*)(size_t)a_mxw = tm; }
    CB();
    float M;
    _Pragma("nounroll") for (unsigned spin = 0; spin < (1u << 21); ++spin) {
      CB();
      const f32x4 ma = *(const LAS f32x4*)(size_t)a_mxr, mb = *(const LAS f32x4*)(size_t)(a_mxr + 16);
      M = vmax3(vmax3(ma[0], ma[1], ma[2]), vmax3(ma[3], mb[0], mb[1]), vmax3(mb[2], mb[3], mb[3]));
      if (!__builtin_amdgcn_ballot_w64(asu(M) == MX_EMPTY)) break;
    }
    { const float ml2 = M * -1.4426950408889634f;
      const f32x2 ea = sa * 1.4426950408889634f + ml2, eb = sb * 1.4426950408889634f + ml2;
      const v2u pw = {pkbf(f32x2{__builtin_amdgcn_exp2f(ea[0]), __builtin_amdgcn_exp2f(ea[1])}),
                      pkbf(f32x2{__builtin_amdgcn_exp2f(eb[0]), __builtin_amdgcn_exp2f(eb[1])})};
      if (n < 4) *(LAS v2u*)(size_t)a_pw = pw; }
    FLAG(F_P);
    v4u pf[4];
    poll8_read4<F_P, L_P, 64>(lz, a_qp, pf);
    f32x4 oc = {0.f, 0.f, 0.f, 0.f};
#pragma unroll
    for (int ks = 0; ks < 4; ++ks) oc = mfma(pf[ks], v4u{vt[2 * ks].x, vt[2 * ks].y, vt[2 * ks + 1].x, vt[2 * ks + 1].y}, oc);
    gst_quad(rs, ost, g4 < 2 ? (g4 == 0 ? oc[0] : oc[1]) : (g4 == 2 ? oc[2] : oc[3]), tag, lane);
    if (w == 3) {
      f32x4 lc = {0.f, 0.f, 0.f, 0.f};
#pragma unroll
      for (int ks = 0; ks < 4; ++ks) lc = mfma(v4u{0x3F803F80u, 0x3F803F80u, 0x3F803F80u, 0x3F803F80u}, pf[ks], lc);
      if (lane < 4) gst4(rs, mst, M, lc[0], M, lc[0], tag);
    }
    if (w < 2) {
      _Pragma("unroll") for (int i = 0; i < OD; ++i) __builtin_amdgcn_s_sleep(8);
      poll8<F_TO, 2>(lz);
      v4u og[4];
#pragma unroll
      for (int k = 0; k < 4; ++k) og[k] = gld4(rs, goff, 4096 * k);
      _Pragma("nounroll") for (unsigned spin = 0; spin < SPIN_CAP; ++spin) {
        bool bad[4];
#pragma unroll
        for (int k = 0; k < 4; ++k) bad[k] = ubad(og[k], tag);
        if (!__builtin_amdgcn_ballot_w64(bad[0] | bad[1] | bad[2] | bad[3])) break;
        __builtin_amdgcn_s_sleep(RSLP);
        CB();
#pragma unroll
        for (int k = 0; k < 4; ++k) if (bad[k]) og[k] = gld4(rs, goff, 4096 * k);
      }
      f32x4 ov[4];
#pragma unroll
      for (int k = 0; k < 4; ++k) ov[k] = uval(og[k]);
      asm volatile("" : "+v"(ov[0]), "+v"(ov[1]), "+v"(ov[2]), "+v"(ov[3]));
      unsigned wt[4], a_xow = lz + L_XO + w * 128 + (lane & 7) * 16 + g4 * 4;
      asm volatile("" : "+v"(a_xow));
      pollb_read4w<1, L_W, 0>(lz + F_W, lz + rl * 4, wt);
      f32x4 o4 = (ov[0] * asf(wt[0]) + ov[1] * asf(wt[1])) + (ov[2] * asf(wt[2]) + ov[3] * asf(wt[3]));
#pragma unroll
      for (int i = 0; i < 4; ++i) o4[i] += dppm<ROR8>(o4[i]);
      auto s01 = __builtin_amdgcn_permlane16_swap(asu(o4[0]), asu(o4[1]), false, false);
      auto s23 = __builtin_amdgcn_permlane16_swap(asu(o4[2]), asu(o4[3]), false, false);
      auto sf = __builtin_amdgcn_permlane32_swap(asu(asf(s01[0]) + asf(s01[1])), asu(asf(s23[0]) + asf(s23[1])), false, false);
      const float of = asf(sf[0]) + asf(sf[1]);
      *(LAS unsigned*)(size_t)a_xow = pkbf(f32x2{of, of});
      FLAG(F_XO);
    } else if (w == 4) {
      _Pragma("unroll") for (int i = 0; i < 2 * OD - MLE; ++i) __builtin_amdgcn_s_sleep(4);
      poll8<F_TO, 2>(lz);
      v4u ml = gld4(rs, goff);
      _Pragma("nounroll") for (unsigned spin = 0; spin < SPIN_CAP; ++spin) {
        const bool bad = ubad(ml, tag);
        if (!__builtin_amdgcn_ballot_w64(bad)) break;
        __builtin_amdgcn_s_sleep(RSLP);
        CB();
        if (bad) ml = gld4(rs, goff);
      }
      const float m = gval(ml.x);
      float mx = oct_max_a(m);
      mx = vmax3(mx, dppm<ROR8>(mx), mx);
      mx = x16_max_a(mx);
      const float wt = __builtin_amdgcn_exp2f((m - mx) * 1.4426950408889634f);
      float ls = oct_sum(wt * gval(ml.y));
      ls += dppm<ROR8>(ls);
      auto t = __builtin_amdgcn_permlane16_swap(asu(ls), asu(ls), false, false);
      if (lane < 32) ((LAS float*)(L + L_W))[lane] = wt * __builtin_amdgcn_rcpf(asf(t[0]) + asf(t[1]));
      flag_off<F_W>(lz, one);
    } else if (w == 5) {
      poll8<F_TH, 2>(lz);
      v4u hg = gld4(rs, goff);
      _Pragma("nounroll") for (unsigned spin = 0; spin < SPIN_CAP; ++spin) {
        const bool bad = ubad(hg, tag);
        if (!__builtin_amdgcn_ballot_w64(bad)) break;
        __builtin_amdgcn_s_sleep(RSLP);
        CB();
        if (bad) hg = gld4(rs, goff);
      }
      f32x4 hv = uval(hg);
#pragma unroll
      for (int i = 0; i < 4; ++i) hv[i] = x16_sum(hv[i]);
      const f32x2 hp = g4 & 1 ? f32x2{hv[2], hv[3]} : f32x2{hv[0], hv[1]};
      const f32x2 u2 = unpk(pkbf(unpk(pkbf(hp)) + unpk(g4 & 1 ? gaux.y : gaux.x)));
      const f32x2 z = u2 * (u2 * u2 * (0.044715f * -2.3022081981f) + -2.3022081981f);
      const unsigned hb = pkbf(f32x2{u2[0] * __builtin_amdgcn_rcpf(1.f + __builtin_amdgcn_exp2f(z[0])),
                                     u2[1] * __builtin_amdgcn_rcpf(1.f + __builtin_amdgcn_exp2f(z[1]))});
      const int slot = (n < 7 ? 4 * n : n < 14 ? 4 * n + 4 : n == 14 ? 28 : 60) + 2 * g4;
      const unsigned hz = n < 14 ? hb : 0u;
      if (lane < 32) *(LAS v2u*)(L + L_XH + slot * 4) = v2u{__builtin_amdgcn_perm(hz, hz, 0x01000100u), __builtin_amdgcn_perm(hz, hz, 0x03020302u)};
      flag_off<F_XH>(lz, one);
    } else if (w == 6) {
      const int col = (b >> 6) * 256 + ((b >> 4) & 3) * 64 + ((b >> 3) & 1) * 32 + (b & 7) + 8 * (lane & 3);
      float xb2 = asf((unsigned)((const GAS u16*)x)[col] << 16) + asf((unsigned)((const GAS u16*)b2)[col] << 16);
      GAS u16* op = (GAS u16*)out + col;
      gu64* fp = (gu64*)(ws + WS_FG + b * 2048) + lane;
      Tag8 t8 = make_tag8(rec);
      t8.tb <<= 24;
      asm volatile("" : "+v"(xb2), "+v"(op), "+v"(fp), "+v"(t8.th), "+v"(t8.tb));
      poll8<F_FRED, 1>(lz);
      unsigned long long zero = 0; asm volatile("" : "+v"(zero));
      unsigned long long f[4];
#define FIN_LD(k) __hip_atomic_fetch_or(fp + 64 * (k), zero, RLX_AGENT)
#pragma unroll
      for (int k = 0; k < 4; ++k) f[k] = FIN_LD(k);
      _Pragma("nounroll") for (unsigned spin = 0; spin < SPIN_CAP; ++spin) {
        unsigned bad[4];
#pragma unroll
        for (int k = 0; k < 4; ++k) bad[k] = ((unsigned)(f[k] >> 32) ^ t8.th) | (((unsigned)f[k] ^ t8.tb) >> 24);
        if (!__builtin_amdgcn_ballot_w64((bad[0] | bad[1] | bad[2] | bad[3]) != 0)) break;
#pragma unroll
        for (int k = 0; k < 4; ++k) if (bad[k]) f[k] = FIN_LD(k);
      }
      float sm = (asf((unsigned)f[0] << 8) + asf((unsigned)f[1] << 8)) + (asf((unsigned)f[2] << 8) + asf((unsigned)f[3] << 8));
      sm += dppm<ROR4>(sm); sm += dppm<ROR8>(sm); sm = x16_sum(sm);
      if (lane < 4) *op = (u16)bfbits(sm + xb2);
      if (b == 0 && lane == 0) { const v2u nx = *(const LAS v2u*)(L + R_NEXT); __hip_atomic_store((gu64*)ws, ((unsigned long long)nx.y << 32) | nx.x, RLX_AGENT); }
    }
  } else {
    const size_t kvb = (((size_t)g * SK + (SK - WIN) + sp * KPS + 16 * w) * DH) * 2;
    const char* kb = (const char*)kc + kvb; const char* vb = (const char*)vc + kvb;
    const unsigned kvl = lane * 16;
    const int cs = w & 3, hf = w >> 2;
    const char* p2 = (const char*)(w2 + (size_t)(rg * 56 + hf * 28) * DM + cg * 256 + cs * 64);
    const char* po = (const char*)(wo + (size_t)(rg * 64 + hf * 32) * DM + cg * 256 + cs * 64);
    const unsigned l2 = rl * 2048 + (lane & 7) * 16;
    const unsigned l2l = rl < 4 ? l2 + 24 * 2048 : l2;
    const char* p1 = has1 ? (const char*)(w1 + (size_t)(rg1 * 256 + w * 32) * FF + cg1 * 64) : p2;
    const unsigned l1 = has1 ? rl * (FF * 2) + (lane & 7) * 16 : l2;
    const int st1 = has1 ? 8 * FF * 2 : 0;
    if (w == 5) { ((LAS unsigned*)L)[lane] = 0u; if (lane < 32) ((LAS unsigned*)(L + L_MX))[lane] = MX_EMPTY; LGKM0(); }
    PIN();
    __builtin_amdgcn_s_barrier();
    PIN();
    __builtin_amdgcn_s_setprio(3);
    if (w == 0) {
      unsigned u[8] = {xa0.x, xa0.y, xa0.z, xa0.w, xa1.x, xa1.y, xa1.z, xa1.w};
      const LnStat t1 = ln_stat(u);
#pragma unroll
      for (int i = 0; i < 8; ++i) u[i] = pkbf(unpk(pkbf(unpk(u[i]) - t1.mean)) * t1.rinv);
      *(LAS v4u*)(L + L_XN + lane * 32) = v4u{u[0], u[1], u[2], u[3]};
      *(LAS v4u*)(L + L_XN + lane * 32 + 16) = v4u{u[4], u[5], u[6], u[7]};
      flag_off<R_LN1>(lz, one);
      xa0 = v4u{u[0], u[1], u[2], u[3]}; xa1 = v4u{u[4], u[5], u[6], u[7]};
    }
    else { _Pragma("unroll") for (int i = 0; i < SD; ++i) __builtin_amdgcn_s_sleep(4); }
    const __amdgpu_buffer_rsrc_t rs = __builtin_amdgcn_make_buffer_rsrc(ws, 0, WS_END, 0x00020000);
    v4u msk = sel_operand(0xFFFFu, n);
    asm volatile("" : "+v"(msk));
    v4u d[20]; v4u bo[4]; Tag tag; Tag8 t8;
    f32x4 acc = {0.f, 0.f, 0.f, 0.f};
    const unsigned a_kv = lz + L_K + 4096 * w + 256 * g4;
    static_for<-1, 20>([&](auto c_) {
      constexpr int c = decltype(c_)::value;
      if constexpr (c >= 0) {
        constexpr int kd = slot_kind(c), j = slot_idx(c);
        if (kd < 2) *(LAS v4u*)(size_t)(a_kv + kd * 32768 + 1024 * j + 16 * (n ^ ((g4 << 2) | j))) = d[c];
        else if (kd == 2) acc = mfma(d[c], bo[j], acc);
        else if (top_slot(c) > top_slot(c - 1) || c == TRIG_O || c == TRIG_H) LANDED(d[c]);
        if (kd == 0 && j == 3) FLAG(F_K);
        if (kd == 1 && j == 3) { FLAG(F_V); }
        if (c == TRIG_Q) FLAG(F_TQ);
        if (c == TRIG_O) FLAG(F_TO);
        if (c == TRIG_H) FLAG(F_TH);
      }
      PIN();
      static_for<(c < 0 ? 0 : top_slot(c - 1) + 1), top_slot(c) + 1>([&](auto i_) {
        constexpr int i = decltype(i_)::value, kd = slot_kind(i), j = slot_idx(i);
        if (i == HOLD) poll4<F_QI, 1>(lz);
        if (kd == 0) d[i] = ldnt(kb + j * 1024 + kvl);
        else if (kd == 1) d[i] = ldnt(vb + j * 1024 + kvl);
        else if (kd == 2) d[i] = ldnt(p1 + j * st1 + l1);
        else if (kd == 3) d[i] = j < 3 ? ldnt(p2 + j * 16384 + l2) : ldnt(p2 + l2l);
        else d[i] = ldnt(po + j * 16384 + l2);
        PIN();
      });
      if (c == -1 && w == 0) {
        __builtin_amdgcn_s_setprio(0);
        unsigned u[8] = {xa0.x, xa0.y, xa0.z, xa0.w, xa1.x, xa1.y, xa1.z, xa1.w};
        const LnStat t2 = ln_stat(u);
#pragma unroll
        for (int i = 0; i < 8; ++i) u[i] = pkbf(unpk(pkbf(unpk(u[i]) - t2.mean)) * t2.rinv);
        *(LAS v4u*)(L + L_XN + 2048 + lane * 32) = v4u{u[0], u[1], u[2], u[3]};
        *(LAS v4u*)(L + L_XN + 2048 + lane * 32 + 16) = v4u{u[4], u[5], u[6], u[7]};
        flag_off<R_LN2>(lz, one);
        __builtin_amdgcn_s_setprio(3);
      }
      if (c + 1 == W1_FIRST) {
        unsigned yr[4];
        pollw_read4h<R_LN2, L_XN + 2048, 1>(lz, lz + (rg1 * 256 + w * 32 + rl) * 2, yr);
#pragma unroll
        for (int k = 0; k < 4; ++k) bo[k] = msk & (yr[k] * 0x10001u);
        asm volatile("" : "+v"(bo[0]), "+v"(bo[1]), "+v"(bo[2]), "+v"(bo[3]));
      }
      if (c == TRIG_Q) { const v4u rp = poll_rec<R_EP, 1>(lz); tag = make_tag(rp.y, rp.z); t8 = make_tag8(rp); }
      if (c == W1_LAST) {
        if ((g4 < 2) == (n < 8)) {
#pragma unroll
          for (int r = 0; r < 4; ++r) *(LAS float*)(L + L_REDB + redw + r * 640 + w * 8) = acc[r];
        }
        FLAG(F_HRED);
        if (w == 7 && has1) {
          v4u dd[4];
          poll8_read4<F_HRED, L_REDB, 16>(lz, lz + lane * 80, dd);
          gst_quad(rs, WS_HG + (rg1 * FF + cg1 * 64 + lane) * 4, sum16(dd), tag, lane);
        }
      }
      PIN();
    });
    const int slot = (g4 & 1) * 8 + (n & 7);
    unsigned a_fx = lz + L_FX + cs * 1024 + lane * 16, a_ff = lz + F_FX + cs, a_x = lz + hf * 128 + rl * 4, a_xf = lz + F_XO + hf;
    int fst = WS_FG + ((cg * 64 + cs * 16 + slot) * 64 + rg) * 32 + (n >> 3) * 16;
    asm volatile("" : "+v"(a_fx), "+v"(a_ff), "+v"(a_x), "+v"(a_xf), "+v"(fst));
    __builtin_amdgcn_s_setprio(1);
    unsigned xm[4];
    acc = f32x4{0.f, 0.f, 0.f, 0.f};
    pollb_read4w<1, L_XH, 1>(lz + F_XH, a_x, xm);
#pragma unroll
    for (int i = 0; i < 4; ++i) acc = mfma(d[12 + i], msk & xm[i], acc);
    pollb_read4w<1, L_XO, 0>(a_xf, a_x, xm);
    FLAG(F_FRED);
#pragma unroll
    for (int i = 0; i < 4; ++i) acc = mfma(d[16 + i], msk & xm[i], acc);
    if (hf == 1) {
      *(LAS f32x4*)(size_t)a_fx = acc;
      flag_at(a_ff, one);
    } else {
      acc += __builtin_bit_cast(f32x4, poll1_read1<0>(a_ff, a_fx));
      f32x4 fo;
#pragma unroll
      for (int r = 0; r < 4; ++r) {
        auto sw = __builtin_amdgcn_permlane32_swap(asu(acc[r]), asu(acc[r]), false, false);
        fo[r] = acc[r] + dppm<ROR8>(asf(sw[1]));
      }
      const float a2 = dppm<ROR8>(fo[2]), a3 = dppm<ROR8>(fo[3]);
      const v4u fv = {g8pk(n < 8 ? fo[0] : a2, t8), t8.th, g8pk(n < 8 ? fo[1] : a3, t8), t8.th};
      if (g4 < 2) __builtin_amdgcn_raw_buffer_store_b128(fv, rs, fst, 0, 16);
    }
  }
}

extern "C" void kernel_launch(void* const* d_in, const int* in_sizes, int n_in, void* d_out, int out_size, void* d_ws, size_t ws_size,
                              hipStream_t stream) {
  fused_kernel<<<NWG, NTHR, 0, stream>>>((const u16*)d_in[0], (const u16*)d_in[1], (const u16*)d_in[2], (const u16*)d_in[3], (const u16*)d_in[4],
                                         (const u16*)d_in[6], (const u16*)d_in[7], (const u16*)d_in[8], (const u16*)d_in[9], (u16*)d_out,
                                         (unsigned char*)d_ws);
}
```

```cpp
#include <hip/hip_runtime.h>
#include <hip/hip_bf16.h>
#include <stdint.h>

typedef unsigned v4u __attribute__((ext_vector_type(4)));
typedef unsigned v2u __attribute__((ext_vector_type(2)));
typedef float f32x4 __attribute__((ext_vector_type(4)));
typedef float f32x2 __attribute__((ext_vector_type(2)));
typedef short s16x4 __attribute__((ext_vector_type(4)));
typedef __bf16 bf16x8 __attribute__((ext_vector_type(8)));
typedef __bf16 bf16x2 __attribute__((ext_vector_type(2)));
typedef unsigned short u16;
#define LAS __attribute__((address_space(3)))
#define GAS __attribute__((address_space(1)))
typedef GAS unsigned long long gu64;
#define RLX_AGENT __ATOMIC_RELAXED, __HIP_MEMORY_SCOPE_AGENT
#define SB() __builtin_amdgcn_sched_barrier(0)
#define CB() asm volatile("" ::: "memory")
#define PIN() do { CB(); SB(); } while (0)
#define LGKM0() asm volatile("s_waitcnt lgkmcnt(0)" ::: "memory")

constexpr int DEPTH = 3;
constexpr int DKV = 4;
constexpr int SD = 8;
constexpr int TRIG_Q = 6;
constexpr int TRIG_O = 12;
constexpr int TRIG_H = 14;
constexpr int OD = 4;
constexpr int MLE = 1;
constexpr int RSLP = 3;

constexpr int DM = 1024, SK = 65536, WIN = 4096, DH = 128, FF = 3584;
constexpr int NWG = 256, NTHR = 1024;
constexpr int NSP = 32, KPS = WIN / NSP;
constexpr int WS_QG = 4096;
constexpr int WS_HG = WS_QG + 4 * 4096 * 4;
constexpr int WS_ML = WS_HG + 4 * FF * 4;
constexpr int WS_OG = WS_ML + 8 * NSP * 128;
constexpr int WS_FG = WS_OG + 32 * NSP * 128 * 4;
constexpr int WS_END = WS_FG + 256 * 64 * 32;
constexpr int F_QRED = 0, F_Q = 8, F_P = 16, F_K = 24, F_V = 32, F_TQ = 40, F_TO = 48, F_TH = 56, F_HRED = 64, F_XO = 72, F_XH = 80,
              F_FX = 88  , F_FRED = 96, F_W = 104, F_QI = 160, R_NEXT = 168, R_LN1 = 112, R_LN2 = 128, R_EP = 144;
constexpr int L_MX = 256;
constexpr int L_REDA = 384;
constexpr int L_REDB = L_REDA + 5120;
constexpr int L_FX = L_REDB + 5120;
constexpr int L_Q = L_FX + 4096;
constexpr int L_P = L_Q + 1088;
constexpr int L_XO = L_P + 1088;
constexpr int L_XH = L_XO + 256;
constexpr int L_W = L_XH + 256;
constexpr int L_XN = L_W + 128;
constexpr int L_K = L_XN + 4096;
constexpr int L_V = L_K + 32768;
constexpr int L_END = L_V + 32768;
constexpr int LDS_BYTES = 96 * 1024;
static_assert(L_END <= LDS_BYTES && (L_K % 16) == 0 && (L_Q % 16) == 0 && (L_FX % 16) == 0 && (L_XO % 16) == 0, "lds");
constexpr unsigned MX_EMPTY = 0x7F7FFFFFu;
constexpr unsigned SPIN_CAP = 1u << 19;

__device__ __forceinline__ float asf(unsigned u) { return __builtin_bit_cast(float, u); }
__device__ __forceinline__ unsigned asu(float f) { return __builtin_bit_cast(unsigned, f); }
__device__ __forceinline__ float blo(unsigned w) { return asf(w << 16); }
__device__ __forceinline__ float bhi(unsigned w) { return asf(w & 0xffff0000u); }
__device__ __forceinline__ unsigned bfbits(float f) { return (unsigned)__builtin_bit_cast(u16, (__bf16)f); }
__device__ __forceinline__ float rbf(float f) { return asf(bfbits(f) << 16); }
__device__ __forceinline__ unsigned pkbf(f32x2 v) { return __builtin_bit_cast(unsigned, __builtin_convertvector(v, bf16x2)); }
__device__ __forceinline__ f32x2 unpk(unsigned w) { return f32x2{blo(w), bhi(w)}; }
__device__ __forceinline__ float dot2(unsigned a, unsigned b, float c) {
  return __builtin_amdgcn_fdot2_f32_bf16(__builtin_bit_cast(bf16x2, a), __builtin_bit_cast(bf16x2, b), c, false);
}
__device__ __forceinline__ v4u ldnt(const void* p) { return __builtin_nontemporal_load((const GAS v4u*)p); }
__device__ __forceinline__ f32x4 mfma(v4u a, v4u b, f32x4 c) {
  return __builtin_amdgcn_mfma_f32_16x16x32_bf16(__builtin_bit_cast(bf16x8, a), __builtin_bit_cast(bf16x8, b), c, 0, 0, 0);
}
template <int CTRL> __device__ __forceinline__ float dppz(float x) { return asf(__builtin_amdgcn_update_dpp(0, asu(x), CTRL, 0xf, 0xf, true)); }
template <int CTRL> __device__ __forceinline__ float dppm(float x) { return asf(__builtin_amdgcn_mov_dpp(asu(x), CTRL, 0xf, 0xf, true)); }
__device__ __forceinline__ float wave_sum(float v) {
  v += dppz<0x111>(v); v += dppz<0x112>(v); v += dppz<0x114>(v); v += dppz<0x118>(v);
  v += asf(__builtin_amdgcn_update_dpp(0, asu(v), 0x142, 0xa, 0xf, false));
  v += asf(__builtin_amdgcn_update_dpp(0, asu(v), 0x143, 0xc, 0xf, false));
  return asf(__builtin_amdgcn_readlane(asu(v), 63));
}
__device__ __forceinline__ float x16_sum(float x) {
  auto s = __builtin_amdgcn_permlane16_swap(asu(x), asu(x), false, false);
  x = asf(s[0]) + asf(s[1]);
  auto t = __builtin_amdgcn_permlane32_swap(asu(x), asu(x), false, false);
  return asf(t[0]) + asf(t[1]);
}
constexpr int XOR1 = 0xB1, XOR2 = 0x4E, ROR4 = 0x124, ROR8 = 0x128;
__device__ __forceinline__ float oct_sum(float v) {
  v += dppm<XOR1>(v); v += dppm<XOR2>(v); v += dppm<0x141>(v); return v;
}
__device__ __forceinline__ float vmax3(float a, float b, float c) { float r; asm("v_max3_f32 %0, %1, %2, %3" : "=v"(r) : "v"(a), "v"(b), "v"(c)); return r; }
__device__ __forceinline__ float x16_max_a(float x) {
  float t;
  asm volatile("v_mov_b32 %1, %0\n\ts_nop 1\n\tv_permlane16_swap_b32 %0, %1\n\tv_max_f32 %0, %0, %1\n\tv_mov_b32 %1, %0\n\ts_nop 1\n\t"
      "v_permlane32_swap_b32 %0, %1\n\tv_max_f32 %0, %0, %1" : "+v"(x), "=&v"(t));
  return x;
}
__device__ __forceinline__ float oct_max_a(float x) {
  asm volatile("s_nop 1\n\tv_max_f32_dpp %0, %0, %0 quad_perm:[1,0,3,2] row_mask:0xf bank_mask:0xf\n\ts_nop 1\n\t"
      "v_max_f32_dpp %0, %0, %0 quad_perm:[2,3,0,1] row_mask:0xf bank_mask:0xf\n\ts_nop 1\n\t"
      "v_max_f32_dpp %0, %0, %0 row_half_mirror row_mask:0xf bank_mask:0xf" : "+v"(x));
  return x;
}
struct Tag { unsigned p[4], c[4]; };
__device__ __forceinline__ Tag make_tag(unsigned pa, unsigned pb) {
  pa = __builtin_amdgcn_readfirstlane(pa); pb = __builtin_amdgcn_readfirstlane(pb);
  return Tag{{pa, pa >> 10, pb, pb >> 10}, {pa << 22, (pa >> 10) << 22, pb << 22, (pb >> 10) << 22}};
}
__device__ __forceinline__ unsigned gpk(float v, unsigned piece) { return __builtin_amdgcn_alignbit(piece, asu(v) + 0x200u, 10); }
__device__ __forceinline__ bool ubad(v4u u, const Tag& t) { return ((u.x ^ t.c[0]) | (u.y ^ t.c[1]) | (u.z ^ t.c[2]) | (u.w ^ t.c[3])) > 0x3FFFFFu; }
__device__ __forceinline__ float gval(unsigned d) { return asf(d << 10); }
__device__ __forceinline__ f32x4 uval(v4u u) { return __builtin_bit_cast(f32x4, u << 10); }
__device__ __forceinline__ v4u gld4(__amdgpu_buffer_rsrc_t rs, int off, int soff = 0) { return __builtin_amdgcn_raw_buffer_load_b128(rs, off, soff, 16); }
__device__ __forceinline__ void gst4(__amdgpu_buffer_rsrc_t rs, int off, float a, float b, float c, float d, const Tag& t) {
  const v4u v = {gpk(a, t.p[0]), gpk(b, t.p[1]), gpk(c, t.p[2]), gpk(d, t.p[3])};
  __builtin_amdgcn_raw_buffer_store_b128(v, rs, off, 0, 16);
}
struct Tag8 { unsigned th, tb; };
__device__ __forceinline__ Tag8 make_tag8(v4u rec) { return Tag8{(rec.w >> 8) | ((rec.x >> 8) << 24), rec.w & 255u}; }
__device__ __forceinline__ unsigned g8pk(float v, Tag8 t) { return __builtin_amdgcn_alignbit(t.tb, asu(v) + 0x80u, 8); }
__device__ __forceinline__ void gst_quad(__amdgpu_buffer_rsrc_t rs, int off, float v, const Tag& t, int lane) {
  const float v1 = dppm<0x55>(v), v2 = dppm<0xAA>(v), v3 = dppm<0xFF>(v);
  if (!(lane & 3)) gst4(rs, off, v, v1, v2, v3, t);
}

#define POLL_HEAD "s_mov_b32 %[c], 0x1000000\n1:\n\t"
#define POLL_TAIL(SLP) "s_cbranch_vccz 2f\n\t" SLP "s_sub_u32 %[c], %[c], 1\n\ts_cbranch_scc0 1b\n2:\n\ts_waitcnt lgkmcnt(0)"
template <int FO, int SLP> __device__ __forceinline__ void poll8(unsigned z) {
  unsigned long long f; unsigned c;
  asm volatile(POLL_HEAD "ds_read_b64 %[f], %[z] offset:%[fo]\n\ts_waitcnt lgkmcnt(0)\n\tv_cmp_ne_u64 vcc, %[f], %[o]\n\t" POLL_TAIL("s_sleep %[s]\n\t")
               : [f] "=&v"(f), [c] "=&s"(c) : [z] "v"(z), [fo] "n"(FO), [o] "s"(0x0101010101010101ull), [s] "n"(SLP) : "vcc", "scc", "memory");
}
template <int FO, int SLP> __device__ __forceinline__ void poll4(unsigned z) {
  unsigned f, c;
  asm volatile(POLL_HEAD "ds_read_b32 %[f], %[z] offset:%[fo]\n\ts_waitcnt lgkmcnt(0)\n\tv_cmp_ne_u32 vcc, %[o], %[f]\n\t" POLL_TAIL("s_sleep %[s]\n\t")
               : [f] "=&v"(f), [c] "=&s"(c) : [z] "v"(z), [fo] "n"(FO), [o] "s"(0x01010101u), [s] "n"(SLP) : "vcc", "scc", "memory");
}
#define RD4 "ds_read_b128 %[d0], %[a] offset:%[o0]\n\tds_read_b128 %[d1], %[a] offset:%[o1]\n\tds_read_b128 %[d2], %[a] offset:%[o2]\n\tds_read_b128 %[d3], %[a] offset:%[o3]\n\t"
template <int FO, int DO, int ST> __device__ __forceinline__ void poll8_read4(unsigned z, unsigned da, v4u (&d)[4]) {
  unsigned long long f; unsigned c;
  asm volatile("ds_read_b64 %[f], %[z] offset:%[fo]\n\t" RD4 "s_waitcnt lgkmcnt(4)\n\tv_cmp_ne_u64 vcc, %[f], %[o]\n\ts_cbranch_vccz 3f\n\t"
               POLL_HEAD "s_sleep 0\n\tds_read_b64 %[f], %[z] offset:%[fo]\n\ts_waitcnt lgkmcnt(0)\n\tv_cmp_ne_u64 vcc, %[f], %[o]\n\t"
               "s_cbranch_vccz 2f\n\ts_sub_u32 %[c], %[c], 1\n\ts_cbranch_scc0 1b\n2:\n\t" RD4 "3:\n\ts_waitcnt lgkmcnt(0)"
               : [f] "=&v"(f), [c] "=&s"(c), [d0] "=&v"(d[0]), [d1] "=&v"(d[1]), [d2] "=&v"(d[2]), [d3] "=&v"(d[3])
               : [z] "v"(z), [a] "v"(da), [fo] "n"(FO), [o0] "n"(DO), [o1] "n"(DO + ST), [o2] "n"(DO + 2 * ST), [o3] "n"(DO + 3 * ST),
                 [o] "s"(0x0101010101010101ull) : "vcc", "scc", "memory");
}
template <int DO, int ST, int SLP> __device__ __forceinline__ void poll4_read4(unsigned fa, unsigned da, v4u (&d)[4]) {
  unsigned f, c;
  asm volatile("ds_read_b32 %[f], %[fa]\n\t" RD4 "s_waitcnt lgkmcnt(4)\n\tv_cmp_ne_u32 vcc, %[o], %[f]\n\ts_cbranch_vccz 3f\n\t"
               POLL_HEAD "s_sleep %[s]\n\tds_read_b32 %[f], %[fa]\n\ts_waitcnt lgkmcnt(0)\n\tv_cmp_ne_u32 vcc, %[o], %[f]\n\t"
               "s_cbranch_vccz 2f\n\ts_sub_u32 %[c], %[c], 1\n\ts_cbranch_scc0 1b\n2:\n\t" RD4 "3:\n\ts_waitcnt lgkmcnt(0)"
               : [f] "=&v"(f), [c] "=&s"(c), [d0] "=&v"(d[0]), [d1] "=&v"(d[1]), [d2] "=&v"(d[2]), [d3] "=&v"(d[3])
               : [fa] "v"(fa), [a] "v"(da), [o0] "n"(DO), [o1] "n"(DO + ST), [o2] "n"(DO + 2 * ST), [o3] "n"(DO + 3 * ST),
                 [o] "s"(0x01010101u), [s] "n"(SLP) : "vcc", "scc", "memory");
}
template <int SLP> __device__ __forceinline__ v4u poll1_read1(unsigned fa, unsigned da) {
  unsigned f, c; v4u d;
  asm volatile(POLL_HEAD "ds_read_u8 %[f], %[fa]\n\tds_read_b128 %[d], %[a]\n\ts_waitcnt lgkmcnt(1)\n\tv_cmp_eq_u32 vcc, 0, %[f]\n\t" POLL_TAIL("s_sleep %[s]\n\t")
               : [f] "=&v"(f), [c] "=&s"(c), [d] "=&v"(d) : [fa] "v"(fa), [a] "v"(da), [s] "n"(SLP) : "vcc", "scc", "memory");
  return d;
}
template <int NB, int DO, int SLP> __device__ __forceinline__ void pollb_read4w(unsigned fa, unsigned da, unsigned (&d)[4]) {
  unsigned f, c;
#define RD4W "ds_read_b32 %[d0], %[a] offset:%[o0]\n\tds_read_b32 %[d1], %[a] offset:%[o1]\n\tds_read_b32 %[d2], %[a] offset:%[o2]\n\tds_read_b32 %[d3], %[a] offset:%[o3]\n\t"
#define POLLB(RD) asm volatile(RD " %[f], %[fa]\n\t" RD4W "s_waitcnt lgkmcnt(4)\n\tv_cmp_ne_u32 vcc, %[o], %[f]\n\ts_cbranch_vccz 3f\n\t" \
               POLL_HEAD "s_sleep %[s]\n\t" RD " %[f], %[fa]\n\ts_waitcnt lgkmcnt(0)\n\tv_cmp_ne_u32 vcc, %[o], %[f]\n\t" \
               "s_cbranch_vccz 2f\n\ts_sub_u32 %[c], %[c], 1\n\ts_cbranch_scc0 1b\n2:\n\t" RD4W "3:\n\ts_waitcnt lgkmcnt(0)" \
               : [f] "=&v"(f), [c] "=&s"(c), [d0] "=&v"(d[0]), [d1] "=&v"(d[1]), [d2] "=&v"(d[2]), [d3] "=&v"(d[3]) \
               : [fa] "v"(fa), [a] "v"(da), [o0] "n"(DO), [o1] "n"(DO + 32), [o2] "n"(DO + 64), [o3] "n"(DO + 96), [o] "s"(NB == 1 ? 1u : 0x0101u), [s] "n"(SLP) \
               : "vcc", "scc", "memory")
  if (NB == 1) POLLB("ds_read_u8"); else POLLB("ds_read_u16");
}
template <int FO, int DO, int SLP> __device__ __forceinline__ void pollw_read4h(unsigned z, unsigned da, unsigned (&d)[4]) {
  unsigned f, c;
#define RD4H "ds_read_u16 %[d0], %[a] offset:%[o0]\n\tds_read_u16 %[d1], %[a] offset:%[o1]\n\tds_read_u16 %[d2], %[a] offset:%[o2]\n\tds_read_u16 %[d3], %[a] offset:%[o3]\n\t"
  asm volatile(POLL_HEAD "ds_read_b32 %[f], %[z] offset:%[fo]\n\t" RD4H "s_waitcnt lgkmcnt(4)\n\tv_cmp_eq_u32 vcc, 0, %[f]\n\t" POLL_TAIL("s_sleep %[s]\n\t")
               : [f] "=&v"(f), [c] "=&s"(c), [d0] "=&v"(d[0]), [d1] "=&v"(d[1]), [d2] "=&v"(d[2]), [d3] "=&v"(d[3])
               : [z] "v"(z), [a] "v"(da), [fo] "n"(FO), [o0] "n"(DO), [o1] "n"(DO + 16), [o2] "n"(DO + 32), [o3] "n"(DO + 48), [s] "n"(SLP) : "vcc", "scc", "memory");
}
template <int RO, int SLP> __device__ __forceinline__ v4u poll_rec(unsigned z) {
  unsigned f, c; v4u d;
  asm volatile(POLL_HEAD "ds_read_b32 %[f], %[z] offset:%[ro]\n\tds_read_b128 %[d], %[z] offset:%[ro]\n\ts_waitcnt lgkmcnt(1)\n\tv_cmp_eq_u32 vcc, 0, %[f]\n\t" POLL_TAIL("s_sleep %[s]\n\t")
               : [f] "=&v"(f), [d] "=&v"(d), [c] "=&s"(c) : [z] "v"(z), [ro] "n"(RO), [s] "n"(SLP) : "vcc", "scc", "memory");
  return d;
}
__device__ __forceinline__ void flag_at(unsigned addr, unsigned one) {
  asm volatile("s_mov_b64 exec, 1\n\tds_write_b8 %0, %1\n\ts_mov_b64 exec, -1" :: "v"(addr), "v"(one) : "memory");
}
template <int OFF> __device__ __forceinline__ void flag_off(unsigned addr, unsigned one) {
  asm volatile("s_mov_b64 exec, 1\n\tds_write_b8 %0, %1 offset:%2\n\ts_mov_b64 exec, -1" :: "v"(addr), "v"(one), "n"(OFF) : "memory");
}
#define FLAG(F) flag_off<F>(lzw, one)
#define LANDED(v) do { asm volatile("" : "+v"(v)); SB(); } while (0)

__device__ __forceinline__ v4u sel_operand(unsigned xb, int n) {
  const unsigned sh = xb << (16 * (n & 1)); const int k = (n & 7) >> 1;
  return v4u{k == 0 ? sh : 0u, k == 1 ? sh : 0u, k == 2 ? sh : 0u, k == 3 ? sh : 0u};
}
struct LnStat { float mean, rinv; };
__device__ __forceinline__ LnStat ln_stat(const unsigned (&u)[8]) {
  float s0 = 0.f, s1 = 0.f;
#pragma unroll
  for (int i = 0; i < 8; i += 2) { s0 = dot2(u[i], 0x3F803F80u, s0); s1 = dot2(u[i + 1], 0x3F803F80u, s1); }
  const float mean = wave_sum(s0 + s1) * (1.f / DM);
  f32x2 q0 = {0.f, 0.f}, q1 = {0.f, 0.f};
#pragma unroll
  for (int i = 0; i < 8; i += 2) { const f32x2 d0 = unpk(u[i]) - mean, d1 = unpk(u[i + 1]) - mean; q0 += d0 * d0; q1 += d1 * d1; }
  q0 += q1;
  const float var = wave_sum(q0[0] + q0[1]) * (1.f / DM);
  const float sd = rbf(__builtin_amdgcn_sqrtf(rbf(rbf(var) + 1e-5f)));
  return LnStat{rbf(mean), __builtin_amdgcn_rcpf(sd)};
}
__device__ __forceinline__ int swz(int row, int ch) { return 256 * row + 16 * (ch ^ (((row & 3) << 2) | ((row >> 2) & 3))); }
__device__ __forceinline__ float sum16(const v4u (&d)[4]) {
  const f32x4 e = (__builtin_bit_cast(f32x4, d[0]) + __builtin_bit_cast(f32x4, d[1])) + (__builtin_bit_cast(f32x4, d[2]) + __builtin_bit_cast(f32x4, d[3]));
  return (e[0] + e[1]) + (e[2] + e[3]);
}
__host__ __device__ constexpr int slot_kind(int i) { return i >= 16 ? 4 : i >= 12 ? 3 : i < 4 ? 0 : i < 5 ? 2 : i < 9 ? 1 : 2; }
__host__ __device__ constexpr int slot_idx(int i) { return i >= 12 ? (i & 3) : i < 4 ? i : i < 5 ? 0 : i < 9 ? i - 5 : i - 8; }
constexpr int HOLD = 9;
__host__ __device__ constexpr int top_slot(int c) {
  const int d0 = c + (c < TRIG_Q ? DKV : DEPTH), d = d0 < 19 ? d0 : 19;
  return c < TRIG_Q ? (d < HOLD - 1 ? d : HOLD - 1) : (d > HOLD - 1 ? d : HOLD - 1);
}
static_assert(TRIG_Q < HOLD, "the q trigger must not depend on a held slot");
constexpr int W1_FIRST = 4, W1_LAST = 11;

template <int V> struct IC { static constexpr int value = V; };
template <int B, int E, class F> __device__ __forceinline__ void static_for(F&& f) {
  if constexpr (B < E) { f(IC<B>{}); static_for<B + 1, E>(f); }
}

__global__ void __launch_bounds__(NTHR, 4) fused_kernel(const u16* x, const u16* kc, const u16* vc, const u16* wq, const u16* wo,
                                                      const u16* w1, const u16* b1, const u16* w2, const u16* b2,
                                                      u16* out, unsigned char* ws) {
  __shared__ __attribute__((aligned(16))) unsigned char lds_raw[LDS_BYTES];
  LAS unsigned char* L = (LAS unsigned char*)lds_raw;
  const int tid = threadIdx.x, lane = tid & 63, wave = __builtin_amdgcn_readfirstlane(tid >> 6);
  const int b = blockIdx.x, g4 = lane >> 4, n = lane & 15, rl = lane >> 3;
  v4u xa0, xa1; unsigned long long ep;
  if (wave == 0) {
    xa0 = *(const GAS v4u*)((const char*)x + (unsigned)(lane * 32)); xa1 = *(const GAS v4u*)((const char*)x + (unsigned)(lane * 32) + 16);
  }
  PIN();
  if (wave >= 1 && wave <= 8) __builtin_amdgcn_s_setprio(3);
  const int cgq = b & 63, rgq = b >> 6;
  const bool has1 = b < 224;
  const int cg1 = (b + 4) % 56, rg1 = has1 ? b / 56 : 0;
  const int cg = (b >> 3) & 3, rg = (b & 7) + 8 * (b >> 5);
  const int hh = (b >> 3) & 1, tkv = (b & 7) + 8 * (b >> 4), g = tkv >> 5, sp = tkv & 31;
  unsigned lz = (unsigned)(size_t)L;
  const bool isA = wave >= 1 && wave <= 8;
  const int w = isA ? wave - 1 : (wave == 0 ? 0 : wave - 8);
  unsigned lzw = lz + w, one = 1u;
  asm volatile("" : "+v"(lz), "+v"(lzw), "+v"(one));
  const int redw = (32 * (g4 & 1) + (n & 7)) * 80 + (g4 >> 1) * 4;

  if (isA) {
    const char* wqb = (const char*)(wq + (size_t)(rgq * 256 + w * 32) * 4096 + cgq * 64);
    const unsigned wql = rl * 8192 + (lane & 7) * 16;
    v4u wv[4];
#pragma unroll
    for (int i = 0; i < 4; ++i) wv[i] = ldnt(wqb + i * 65536 + wql);
    PIN();
    if (w == 1) ep = __hip_atomic_load((gu64*)ws, RLX_AGENT);
    PIN();
    __builtin_amdgcn_s_barrier();
    PIN();
    __builtin_amdgcn_s_setprio(2);
    const __amdgpu_buffer_rsrc_t rs = __builtin_amdgcn_make_buffer_rsrc(ws, 0, WS_END, 0x00020000);
    v4u rec;
    unsigned xr[4];
    v4u msk = sel_operand(0xFFFFu, n);
    asm volatile("" : "+v"(msk));
    pollw_read4h<R_LN1, L_XN, 2>(lz, lz + (rgq * 256 + w * 32 + rl) * 2, xr);
    v4u bq[4];
#pragma unroll
    for (int i = 0; i < 4; ++i) bq[i] = msk & (xr[i] * 0x10001u);
    asm volatile("" : "+v"(bq[0]), "+v"(bq[1]), "+v"(bq[2]), "+v"(bq[3]));
    f32x4 acc = {0.f, 0.f, 0.f, 0.f};
#pragma unroll
    for (int i = 0; i < 4; ++i) acc = mfma(wv[i], bq[i], acc);
    if ((g4 < 2) == (n < 8)) {
#pragma unroll
      for (int r = 0; r < 4; ++r) *(LAS float*)(L + L_REDA + redw + r * 640 + w * 8) = acc[r];
    }
    FLAG(F_QRED);
    if (w == 1) {
      asm volatile("" : "+v"(ep));
      const unsigned long long tg = ep + 1ull;
      const unsigned lo = (unsigned)tg, hi = (unsigned)(tg >> 20);
      rec = v4u{1u | ((unsigned)(tg >> 32) << 8), (lo & 0x3FFu) | ((((lo >> 10) + lo) & 0x3FFu) << 10),
                ((hi + lo) & 0x3FFu) | ((((hi >> 10) + lo) & 0x3FFu) << 10), lo};
      *(LAS v2u*)(L + R_NEXT) = v2u{lo, (unsigned)(tg >> 32)};
      CB();
      *(LAS v4u*)(L + R_EP) = rec;
      v4u d[4];
      const int off = WS_QG + (rgq * 4096 + cgq * 64 + lane) * 4;
      const Tag tg1 = make_tag(rec.y, rec.z);
      poll8_read4<F_QRED, L_REDA, 16>(lz, lz + lane * 80, d);
      gst_quad(rs, off, sum16(d), tg1, lane);
    } else {
      rec = poll_rec<R_EP, 1>(lz);
    }
    const Tag tag = make_tag(rec.y, rec.z);
    const int hq = g * 2 + hh, head = rg >> 1;
    int qoff = WS_QG + ((lane >> 5) * 4096 + g * 1024 + hh * 512 + (w & 3) * 128 + (lane & 31) * 4) * 4;
    unsigned a_qw = lz + L_Q + (w & 3) * 272 + (lane & 31) * 8;
    unsigned a_qp = lz + (n & 3) * 272 + 16 * g4;
    unsigned a_mxw = lz + L_MX + (n & 3) * 32 + w * 4, a_mxr = lz + L_MX + (n & 3) * 32;
    unsigned a_pw = lz + L_P + (n & 3) * 272 + (16 * w + 4 * g4) * 2;
    int ost = WS_OG + (((hq * 4 + g4) * NSP + sp) * 128 + 16 * w + n) * 4;
    int mst = WS_ML + (hq * NSP + sp) * 128 + n * 16;
    asm volatile("" : "+v"(qoff), "+v"(a_qw), "+v"(a_qp), "+v"(a_mxw), "+v"(a_mxr), "+v"(a_pw), "+v"(ost), "+v"(mst));
    int goff; v2u gaux = {0u, 0u};
    if (w < 2) goff = WS_OG + ((head * NSP + rl) * 128 + (rg & 1) * 64 + 32 * w + 4 * (lane & 7)) * 4;
    else if (w == 4) goff = WS_ML + ((head >> 2) * NSP + (lane & 31)) * 128 + (head & 3) * 16;
    else { const int un = n < 14 ? n : 13;
           goff = WS_HG + (g4 * FF + rg * 56 + 4 * un) * 4;
           gaux = *(const GAS v2u*)((const char*)b1 + (unsigned)((rg * 56 + 4 * un) * 2)); }
    asm volatile("" : "+v"(goff));
    poll8<F_TQ, 2>(lz);
    v4u q0, q1;
    if (w < 4) { q0 = gld4(rs, qoff); q1 = gld4(rs, qoff, 2 * 4096 * 4); PIN(); FLAG(F_QI); }
    v4u kf[4];
    poll8<F_K, 1>(lz);
#pragma unroll
    for (int s = 0; s < 4; ++s) kf[s] = *(const LAS v4u*)(L + L_K + swz(16 * w + n, 4 * s + g4));
    asm volatile("" : "+v"(kf[0]), "+v"(kf[1]), "+v"(kf[2]), "+v"(kf[3]));
    v2u vt[8];
    unsigned a_vt[2];
#pragma unroll
    for (int tt = 0; tt < 2; ++tt) a_vt[tt] = L_V + swz(8 * g4 + 4 * tt + (n >> 2), 2 * w + ((n & 3) >> 1)) + 8 * (n & 1);
    asm volatile("" : "+v"(a_vt[0]), "+v"(a_vt[1]));
    poll8<F_V, 1>(lz);
#pragma unroll
    for (int ks = 0; ks < 4; ++ks)
#pragma unroll
      for (int tt = 0; tt < 2; ++tt)
        vt[2 * ks + tt] = __builtin_bit_cast(v2u, __builtin_amdgcn_ds_read_tr16_b64_v4i16((LAS s16x4*)(L + a_vt[tt] + ks * 8192)));
    asm volatile("" : "+v"(vt[0]), "+v"(vt[1]), "+v"(vt[2]), "+v"(vt[3]), "+v"(vt[4]), "+v"(vt[5]), "+v"(vt[6]), "+v"(vt[7]));
    if (w < 4) {
      _Pragma("nounroll") for (unsigned spin = 0; spin < SPIN_CAP; ++spin) {
        const bool bad = ubad(q0, tag) | ubad(q1, tag);
        if (!__builtin_amdgcn_ballot_w64(bad)) break;
        __builtin_amdgcn_s_sleep(RSLP);
        CB();
        if (bad) { q0 = gld4(rs, qoff); q1 = gld4(rs, qoff, 2 * 4096 * 4); }
      }
      const f32x4 qv = uval(q0) + uval(q1);
      f32x4 qa, qb;
#pragma unroll
      for (int i = 0; i < 4; ++i) { auto t = __builtin_amdgcn_permlane32_swap(asu(qv[i]), asu(qv[i]), false, false); qa[i] = asf(t[0]); qb[i] = asf(t[1]); }
      qa += qb;
      *(LAS v2u*)(size_t)a_qw = v2u{pkbf(f32x2{qa[0], qa[1]}), pkbf(f32x2{qa[2], qa[3]})};
      FLAG(F_Q);
    }
    v4u qf[4];
    poll4_read4<L_Q, 64, 0>(lz + F_Q, a_qp, qf);
    f32x4 sc = {0.f, 0.f, 0.f, 0.f};
#pragma unroll
    for (int s = 0; s < 4; ++s) sc = mfma(kf[s], qf[s], sc);
    const unsigned s01 = pkbf(unpk(pkbf(f32x2{sc[0], sc[1]})) * 0.08838834764831845f), s23 = pkbf(unpk(pkbf(f32x2{sc[2], sc[3]})) * 0.08838834764831845f);
    const f32x2 sa = unpk(s01), sb = unpk(s23);
    { const float tm = x16_max_a(vmax3(sa[0], sa[1], vmax3(sb[0], sb[1], sb[1])));
      if (lane < 4) *(LAS float*)(size_t)a_mxw = tm; }
    CB();
    float M;
    _Pragma("nounroll") for (unsigned spin = 0; spin < (1u << 24); ++spin) {
      CB();
      const f32x4 ma = *(const LAS f32x4*)(size_t)a_mxr, mb = *(const LAS f32x4*)(size_t)(a_mxr + 16);
      M = vmax3(vmax3(ma[0], ma[1], ma[2]), vmax3(ma[3], mb[0], mb[1]), vmax3(mb[2], mb[3], mb[3]));
      if (!__builtin_amdgcn_ballot_w64(asu(M) == MX_EMPTY)) break;
    }
    { const f32x2 ea = (sa - M) * 1.4426950408889634f, eb = (sb - M) * 1.4426950408889634f;
      const v2u pw = {pkbf(f32x2{__builtin_amdgcn_exp2f(ea[0]), __builtin_amdgcn_exp2f(ea[1])}),
                      pkbf(f32x2{__builtin_amdgcn_exp2f(eb[0]), __builtin_amdgcn_exp2f(eb[1])})};
      if (n < 4) *(LAS v2u*)(size_t)a_pw = pw; }
    FLAG(F_P);
    v4u pf[4];
    poll8_read4<F_P, L_P, 64>(lz, a_qp, pf);
    if (w == 3) {
      f32x4 lc = {0.f, 0.f, 0.f, 0.f};
#pragma unroll
      for (int ks = 0; ks < 4; ++ks) lc = mfma(v4u{0x3F803F80u, 0x3F803F80u, 0x3F803F80u, 0x3F803F80u}, pf[ks], lc);
      if (lane < 4) gst4(rs, mst, M, lc[0], M, lc[0], tag);
    }
    f32x4 oc = {0.f, 0.f, 0.f, 0.f};
#pragma unroll
    for (int ks = 0; ks < 4; ++ks) oc = mfma(pf[ks], v4u{vt[2 * ks].x, vt[2 * ks].y, vt[2 * ks + 1].x, vt[2 * ks + 1].y}, oc);
    gst_quad(rs, ost, g4 < 2 ? (g4 == 0 ? oc[0] : oc[1]) : (g4 == 2 ? oc[2] : oc[3]), tag, lane);
    if (w < 2) {
      _Pragma("unroll") for (int i = 0; i < OD; ++i) __builtin_amdgcn_s_sleep(8);
      poll8<F_TO, 2>(lz);
      v4u og[4];
#pragma unroll
      for (int k = 0; k < 4; ++k) og[k] = gld4(rs, goff, 4096 * k);
      _Pragma("nounroll") for (unsigned spin = 0; spin < SPIN_CAP; ++spin) {
        bool bad[4];
#pragma unroll
        for (int k = 0; k < 4; ++k) bad[k] = ubad(og[k], tag);
        if (!__builtin_amdgcn_ballot_w64(bad[0] | bad[1] | bad[2] | bad[3])) break;
        __builtin_amdgcn_s_sleep(RSLP);
        CB();
#pragma unroll
        for (int k = 0; k < 4; ++k) if (bad[k]) og[k] = gld4(rs, goff, 4096 * k);
      }
      f32x4 ov[4];
#pragma unroll
      for (int k = 0; k < 4; ++k) ov[k] = uval(og[k]);
      asm volatile("" : "+v"(ov[0]), "+v"(ov[1]), "+v"(ov[2]), "+v"(ov[3]));
      unsigned wt[4], a_xow = lz + L_XO + w * 128 + (lane & 7) * 16 + g4 * 4;
      asm volatile("" : "+v"(a_xow));
      pollb_read4w<1, L_W, 0>(lz + F_W, lz + rl * 4, wt);
      f32x4 o4 = (ov[0] * asf(wt[0]) + ov[1] * asf(wt[1])) + (ov[2] * asf(wt[2]) + ov[3] * asf(wt[3]));
#pragma unroll
      for (int i = 0; i < 4; ++i) o4[i] += dppm<ROR8>(o4[i]);
      auto s01 = __builtin_amdgcn_permlane16_swap(asu(o4[0]), asu(o4[1]), false, false);
      auto s23 = __builtin_amdgcn_permlane16_swap(asu(o4[2]), asu(o4[3]), false, false);
      auto sf = __builtin_amdgcn_permlane32_swap(asu(asf(s01[0]) + asf(s01[1])), asu(asf(s23[0]) + asf(s23[1])), false, false);
      const float of = asf(sf[0]) + asf(sf[1]);
      *(LAS unsigned*)(size_t)a_xow = pkbf(f32x2{of, of});
      FLAG(F_XO);
    } else if (w == 4) {
      _Pragma("unroll") for (int i = 0; i < 2 * OD - MLE; ++i) __builtin_amdgcn_s_sleep(4);
      poll8<F_TO, 2>(lz);
      v4u ml = gld4(rs, goff);
      _Pragma("nounroll") for (unsigned spin = 0; spin < SPIN_CAP; ++spin) {
        const bool bad = ubad(ml, tag);
        if (!__builtin_amdgcn_ballot_w64(bad)) break;
        __builtin_amdgcn_s_sleep(RSLP);
        CB();
        if (bad) ml = gld4(rs, goff);
      }
      const float m = gval(ml.x);
      float mx = oct_max_a(m);
      mx = vmax3(mx, dppm<ROR8>(mx), mx);
      mx = x16_max_a(mx);
      const float wt = __builtin_amdgcn_exp2f((m - mx) * 1.4426950408889634f);
      float ls = oct_sum(wt * gval(ml.y));
      ls += dppm<ROR8>(ls);
      auto t = __builtin_amdgcn_permlane16_swap(asu(ls), asu(ls), false, false);
      if (lane < 32) ((LAS float*)(L + L_W))[lane] = wt * __builtin_amdgcn_rcpf(asf(t[0]) + asf(t[1]));
      flag_off<F_W>(lz, one);
    } else if (w == 5) {
      poll8<F_TH, 2>(lz);
      v4u hg = gld4(rs, goff);
      _Pragma("nounroll") for (unsigned spin = 0; spin < SPIN_CAP; ++spin) {
        const bool bad = ubad(hg, tag);
        if (!__builtin_amdgcn_ballot_w64(bad)) break;
        __builtin_amdgcn_s_sleep(RSLP);
        CB();
        if (bad) hg = gld4(rs, goff);
      }
      f32x4 hv = uval(hg);
#pragma unroll
      for (int i = 0; i < 4; ++i) hv[i] = x16_sum(hv[i]);
      const f32x2 hp = g4 & 1 ? f32x2{hv[2], hv[3]} : f32x2{hv[0], hv[1]};
      const f32x2 u2 = unpk(pkbf(unpk(pkbf(hp)) + unpk(g4 & 1 ? gaux.y : gaux.x)));
      const f32x2 z = u2 * (u2 * u2 * (0.044715f * -2.3022081981f) + -2.3022081981f);
      const unsigned hb = pkbf(f32x2{u2[0] * __builtin_amdgcn_rcpf(1.f + __builtin_amdgcn_exp2f(z[0])),
                                     u2[1] * __builtin_amdgcn_rcpf(1.f + __builtin_amdgcn_exp2f(z[1]))});
      const int slot = (n < 7 ? 4 * n : n < 14 ? 4 * n + 4 : n == 14 ? 28 : 60) + 2 * g4;
      const unsigned hz = n < 14 ? hb : 0u;
      if (lane < 32) *(LAS v2u*)(L + L_XH + slot * 4) = v2u{__builtin_amdgcn_perm(hz, hz, 0x01000100u), __builtin_amdgcn_perm(hz, hz, 0x03020302u)};
      flag_off<F_XH>(lz, one);
    } else if (w == 6) {
      const int col = (b >> 6) * 256 + ((b >> 4) & 3) * 64 + ((b >> 3) & 1) * 32 + (b & 7) + 8 * (lane & 3);
      float xb2 = asf((unsigned)((const GAS u16*)x)[col] << 16) + asf((unsigned)((const GAS u16*)b2)[col] << 16);
      GAS u16* op = (GAS u16*)out + col;
      gu64* fp = (gu64*)(ws + WS_FG + b * 2048) + lane;
      Tag8 t8 = make_tag8(rec);
      t8.tb <<= 24;
      asm volatile("" : "+v"(xb2), "+v"(op), "+v"(fp), "+v"(t8.th), "+v"(t8.tb));
      poll8<F_FRED, 1>(lz);
      unsigned long long zero = 0; asm volatile("" : "+v"(zero));
      unsigned long long f[4];
#define FIN_LD(k) __hip_atomic_fetch_or(fp + 64 * (k), zero, RLX_AGENT)
#pragma unroll
      for (int k = 0; k < 4; ++k) f[k] = FIN_LD(k);
      _Pragma("nounroll") for (unsigned spin = 0; spin < SPIN_CAP; ++spin) {
        unsigned bad[4];
#pragma unroll
        for (int k = 0; k < 4; ++k) bad[k] = ((unsigned)(f[k] >> 32) ^ t8.th) | (((unsigned)f[k] ^ t8.tb) >> 24);
        if (!__builtin_amdgcn_ballot_w64((bad[0] | bad[1] | bad[2] | bad[3]) != 0)) break;
#pragma unroll
        for (int k = 0; k < 4; ++k) if (bad[k]) f[k] = FIN_LD(k);
      }
      float sm = (asf((unsigned)f[0] << 8) + asf((unsigned)f[1] << 8)) + (asf((unsigned)f[2] << 8) + asf((unsigned)f[3] << 8));
      sm += dppm<ROR4>(sm); sm += dppm<ROR8>(sm); sm = x16_sum(sm);
      if (lane < 4) *op = (u16)bfbits(sm + xb2);
      if (b == 0 && lane == 0) { const v2u nx = *(const LAS v2u*)(L + R_NEXT); __hip_atomic_store((gu64*)ws, ((unsigned long long)nx.y << 32) | nx.x, RLX_AGENT); }
    }
  } else {
    if (w == 5) { ((LAS unsigned*)L)[lane] = 0u; if (lane < 32) ((LAS unsigned*)(L + L_MX))[lane] = MX_EMPTY; LGKM0(); }
    PIN();
    __builtin_amdgcn_s_barrier();
    PIN();
    __builtin_amdgcn_s_setprio(3);
    if (w == 0) {
      unsigned u[8] = {xa0.x, xa0.y, xa0.z, xa0.w, xa1.x, xa1.y, xa1.z, xa1.w};
      const LnStat t1 = ln_stat(u);
#pragma unroll
      for (int i = 0; i < 8; ++i) u[i] = pkbf(unpk(pkbf(unpk(u[i]) - t1.mean)) * t1.rinv);
      *(LAS v4u*)(L + L_XN + lane * 32) = v4u{u[0], u[1], u[2], u[3]};
      *(LAS v4u*)(L + L_XN + lane * 32 + 16) = v4u{u[4], u[5], u[6], u[7]};
      flag_off<R_LN1>(lz, one);
      xa0 = v4u{u[0], u[1], u[2], u[3]}; xa1 = v4u{u[4], u[5], u[6], u[7]};
    }
    else { _Pragma("unroll") for (int i = 0; i < SD; ++i) __builtin_amdgcn_s_sleep(4); }
    PIN();
    const size_t kvb = (((size_t)g * SK + (SK - WIN) + sp * KPS + 16 * w) * DH) * 2;
    const char* kb = (const char*)kc + kvb; const char* vb = (const char*)vc + kvb;
    const unsigned kvl = lane * 16;
    const int cs = w & 3, hf = w >> 2;
    const char* p2 = (const char*)(w2 + (size_t)(rg * 56 + hf * 28) * DM + cg * 256 + cs * 64);
    const char* po = (const char*)(wo + (size_t)(rg * 64 + hf * 32) * DM + cg * 256 + cs * 64);
    const unsigned l2 = rl * 2048 + (lane & 7) * 16;
    const unsigned l2l = rl < 4 ? l2 + 24 * 2048 : l2;
    const char* p1 = has1 ? (const char*)(w1 + (size_t)(rg1 * 256 + w * 32) * FF + cg1 * 64) : p2;
    const unsigned l1 = has1 ? rl * (FF * 2) + (lane & 7) * 16 : l2;
    const int st1 = has1 ? 8 * FF * 2 : 0;
    const __amdgpu_buffer_rsrc_t rs = __builtin_amdgcn_make_buffer_rsrc(ws, 0, WS_END, 0x00020000);
    v4u msk = sel_operand(0xFFFFu, n);
    asm volatile("" : "+v"(msk));
    v4u d[20]; v4u bo[4]; Tag tag; Tag8 t8;
    f32x4 acc = {0.f, 0.f, 0.f, 0.f};
    const unsigned a_kv = lz + L_K + 4096 * w + 256 * g4;
    static_for<-1, 20>([&](auto c_) {
      constexpr int c = decltype(c_)::value;
      if constexpr (c >= 0) {
        constexpr int kd = slot_kind(c), j = slot_idx(c);
        if (kd < 2) *(LAS v4u*)(size_t)(a_kv + kd * 32768 + 1024 * j + 16 * (n ^ ((g4 << 2) | j))) = d[c];
        else if (kd == 2) acc = mfma(d[c], bo[j], acc);
        else if (top_slot(c) > top_slot(c - 1) || c == TRIG_O || c == TRIG_H) LANDED(d[c]);
        if (kd == 0 && j == 3) FLAG(F_K);
        if (kd == 1 && j == 3) { FLAG(F_V); }
        if (c == TRIG_Q) FLAG(F_TQ);
        if (c == TRIG_O) FLAG(F_TO);
        if (c == TRIG_H) FLAG(F_TH);
      }
      PIN();
      static_for<(c < 0 ? 0 : top_slot(c - 1) + 1), top_slot(c) + 1>([&](auto i_) {
        constexpr int i = decltype(i_)::value, kd = slot_kind(i), j = slot_idx(i);
        if (i == HOLD) poll4<F_QI, 1>(lz);
        if (kd == 0) d[i] = ldnt(kb + j * 1024 + kvl);
        else if (kd == 1) d[i] = ldnt(vb + j * 1024 + kvl);
        else if (kd == 2) d[i] = ldnt(p1 + j * st1 + l1);
        else if (kd == 3) d[i] = j < 3 ? ldnt(p2 + j * 16384 + l2) : ldnt(p2 + l2l);
        else d[i] = ldnt(po + j * 16384 + l2);
        PIN();
      });
      if (c == -1 && w == 0) {
        __builtin_amdgcn_s_setprio(0);
        unsigned u[8] = {xa0.x, xa0.y, xa0.z, xa0.w, xa1.x, xa1.y, xa1.z, xa1.w};
        const LnStat t2 = ln_stat(u);
#pragma unroll
        for (int i = 0; i < 8; ++i) u[i] = pkbf(unpk(pkbf(unpk(u[i]) - t2.mean)) * t2.rinv);
        *(LAS v4u*)(L + L_XN + 2048 + lane * 32) = v4u{u[0], u[1], u[2], u[3]};
        *(LAS v4u*)(L + L_XN + 2048 + lane * 32 + 16) = v4u{u[4], u[5], u[6], u[7]};
        flag_off<R_LN2>(lz, one);
        __builtin_amdgcn_s_setprio(3);
      }
      if (c + 1 == W1_FIRST) {
        unsigned yr[4];
        pollw_read4h<R_LN2, L_XN + 2048, 1>(lz, lz + (rg1 * 256 + w * 32 + rl) * 2, yr);
#pragma unroll
        for (int k = 0; k < 4; ++k) bo[k] = msk & (yr[k] * 0x10001u);
        asm volatile("" : "+v"(bo[0]), "+v"(bo[1]), "+v"(bo[2]), "+v"(bo[3]));
      }
      if (c == TRIG_Q) { const v4u rp = poll_rec<R_EP, 1>(lz); tag = make_tag(rp.y, rp.z); t8 = make_tag8(rp); }
      if (c == W1_LAST) {
        if ((g4 < 2) == (n < 8)) {
#pragma unroll
          for (int r = 0; r < 4; ++r) *(LAS float*)(L + L_REDB + redw + r * 640 + w * 8) = acc[r];
        }
        FLAG(F_HRED);
        if (w == 7 && has1) {
          v4u dd[4];
          poll8_read4<F_HRED, L_REDB, 16>(lz, lz + lane * 80, dd);
          gst_quad(rs, WS_HG + (rg1 * FF + cg1 * 64 + lane) * 4, sum16(dd), tag, lane);
        }
      }
      PIN();
    });
    const int slot = (g4 & 1) * 8 + (n & 7);
    unsigned a_fx = lz + L_FX + cs * 1024 + lane * 16, a_ff = lz + F_FX + cs, a_x = lz + hf * 128 + rl * 4, a_xf = lz + F_XO + hf;
    int fst = WS_FG + ((cg * 64 + cs * 16 + slot) * 64 + rg) * 32 + (n >> 3) * 16;
    asm volatile("" : "+v"(a_fx), "+v"(a_ff), "+v"(a_x), "+v"(a_xf), "+v"(fst));
    __builtin_amdgcn_s_setprio(1);
    unsigned xm[4];
    acc = f32x4{0.f, 0.f, 0.f, 0.f};
    pollb_read4w<1, L_XH, 1>(lz + F_XH, a_x, xm);
#pragma unroll
    for (int i = 0; i < 4; ++i) acc = mfma(d[12 + i], msk & xm[i], acc);
    pollb_read4w<1, L_XO, 0>(a_xf, a_x, xm);
    FLAG(F_FRED);
#pragma unroll
    for (int i = 0; i < 4; ++i) acc = mfma(d[16 + i], msk & xm[i], acc);
    if (hf == 1) {
      *(LAS f32x4*)(size_t)a_fx = acc;
      flag_at(a_ff, one);
    } else {
      acc += __builtin_bit_cast(f32x4, poll1_read1<0>(a_ff, a_fx));
      f32x4 fo;
#pragma unroll
      for (int r = 0; r < 4; ++r) {
        auto sw = __builtin_amdgcn_permlane32_swap(asu(acc[r]), asu(acc[r]), false, false);
        fo[r] = acc[r] + dppm<ROR8>(asf(sw[1]));
      }
      const float a2 = dppm<ROR8>(fo[2]), a3 = dppm<ROR8>(fo[3]);
      const v4u fv = {g8pk(n < 8 ? fo[0] : a2, t8), t8.th, g8pk(n < 8 ? fo[1] : a3, t8), t8.th};
      if (g4 < 2) __builtin_amdgcn_raw_buffer_store_b128(fv, rs, fst, 0, 16);
    }
  }
}

extern "C" void kernel_launch(void* const* d_in, const int* in_sizes, int n_in, void* d_out, int out_size, void* d_ws, size_t ws_size,
                              hipStream_t stream) {
  fused_kernel<<<NWG, NTHR, 0, stream>>>((const u16*)d_in[0], (const u16*)d_in[1], (const u16*)d_in[2], (const u16*)d_in[3], (const u16*)d_in[4],
                                         (const u16*)d_in[6], (const u16*)d_in[7], (const u16*)d_in[8], (const u16*)d_in[9], (u16*)d_out,
                                         (unsigned char*)d_ws);
}
```
